```python
import torch

def _softmax(x, axis=-1):
    return torch.softmax(x, dim=axis)
def _gelu(x):
    return torch.nn.functional.gelu(x, approximate="tanh")
def _silu(x):
    return torch.nn.functional.silu(x)
def _sigmoid(x):
    return torch.sigmoid(x)
def _layernorm(x, axis=-1, eps=1e-5):
    m = x.mean(dim=axis, keepdim=True)
    v = x.var(dim=axis, keepdim=True, unbiased=False)
    return (x - m) / torch.sqrt(v + eps)
def _scatter_add(tgt, idx, src, axis=0):
    return tgt.clone().index_add_(axis, idx, src)
def _segment_sum(src, seg, n):
    out = torch.zeros((n,) + src.shape[1:], dtype=src.dtype)
    return out.index_add_(0, seg, src)
def _pad(x, pw):
    flat = tuple(v for pair in reversed(pw) for v in pair)
    return torch.nn.functional.pad(x, flat)

kernel_name = 'sequential_preLN__sliding_block__dense_activation_relu__prompt'

def setup_inputs():

    _w = (lambda a: (lambda t: t.to(torch.bfloat16) if t.is_floating_point() else t)(torch.as_tensor(a)))
    def _A_setup():
        B, S, D = 1, 2048, 640
        return {
            "x": _w((lambda *s: torch.randn(*s, dtype=torch.bfloat16) * 0.05)(B, S, D)),
            "wqkv": _w((lambda *s: torch.randn(*s, dtype=torch.bfloat16) * 0.05)(D, 3 * D)),
            "wo": _w((lambda *s: torch.randn(*s, dtype=torch.bfloat16) * 0.05)(D, D)),
        }
    _A_in = _A_setup()

    def _B_setup():
        B, S, D, Dff = 1, 2048, 640, 1728
        return {
            "x": _w((lambda *s: torch.randn(*s, dtype=torch.bfloat16) * 0.05)(B, S, D)),
            "w1": _w((lambda *s: torch.randn(*s, dtype=torch.bfloat16) * 0.05)(D, Dff)),
            "w2": _w((lambda *s: torch.randn(*s, dtype=torch.bfloat16) * 0.05)(Dff, D)),
        }
    _B_in = _B_setup()

    return {
        "A_x": _A_in["x"],
        "A_wqkv": _A_in["wqkv"],
        "A_wo": _A_in["wo"],
        "B_x": _B_in["x"],
        "B_w1": _B_in["w1"],
        "B_w2": _B_in["w2"],
    }

def reference(A_x, A_wqkv, A_wo, B_x, B_w1, B_w2):
    def _A_ref(x, wqkv, wo):
        B, S, D = x.shape
        H = 32; Dh = D // H; W = 256
        qkv = (lambda x, *d: x.permute(*d))((x @ wqkv).reshape(B, S, 3, H, Dh), 2, 0, 3, 1, 4)
        q, k, v = qkv[0], qkv[1], qkv[2]
        scores = torch.einsum("bhqd,bhkd->bhqk", q, k) * (Dh ** -0.5)
        idx = torch.arange(S)
        mask = (idx[:, None] - idx[None, :] >= 0) & (idx[:, None] - idx[None, :] < W)
        scores = torch.where(mask[None, None], scores, -1e9)
        p = _softmax(scores, axis=-1)
        o = torch.einsum("bhqk,bhkd->bhqd", p, v)
        return (lambda x, *d: x.permute(*d))(o, 0, 2, 1, 3).reshape(B, S, D) @ wo

    def _B_ref(x, w1, w2):
        h = torch.einsum("bsd,df->bsf", x, w1)
        zero = torch.zeros_like(h)
        h = torch.maximum(h, zero)
        return torch.einsum("bsf,fd->bsd", h, w2)

    x = A_x
    h = x + _A_ref(_layernorm(x, axis=-1), A_wqkv, A_wo)
    h2 = h + _B_ref(_layernorm(h, axis=-1), B_w1, B_w2)
    return h2
```

```cpp
#include <hip/hip_runtime.h>
#include <hip/hip_bf16.h>
typedef unsigned short u16;
__device__ __forceinline__ float bf2f(u16 b){ return __uint_as_float(((unsigned)b)<<16); }
__device__ __forceinline__ u16 f2bf(float f){ unsigned u=__float_as_uint(f); u += 0x7FFFu + ((u>>16)&1u); return (u16)(u>>16); }
__device__ __forceinline__ float rbf(float f){ return bf2f(f2bf(f)); }

constexpr int S=2048, D=640, H=32, DH=20, W=256, DFF=1728;

__global__ void ln_k(const u16* x, u16* y){
  int row=blockIdx.x; int t=threadIdx.x;
  float v[10]; float s=0;
  for(int i=0;i<10;i++){ v[i]=bf2f(x[row*D+t+64*i]); s+=v[i]; }
  for(int o=32;o>0;o>>=1) s+=__shfl_xor(s,o);
  float m=s/D; float q=0;
  for(int i=0;i<10;i++){ float d=v[i]-m; q+=d*d; }
  for(int o=32;o>0;o>>=1) q+=__shfl_xor(q,o);
  float var=rbf(q/D); float mb=rbf(m);
  float sd=rbf(sqrtf(rbf(var+1e-5f)));
  for(int i=0;i<10;i++){ y[row*D+t+64*i]=f2bf(rbf(v[i]-mb)/sd); }
}
__global__ void gemm_k(const u16* A,const u16* B,u16* C,const u16* R,int M,int N,int K,int mode){
  int n=blockIdx.x*64+threadIdx.x; int m=blockIdx.y;
  if(n>=N) return;
  float acc=0;
  for(int k=0;k<K;k++) acc+=bf2f(A[m*K+k])*bf2f(B[k*N+n]);
  float r=rbf(acc);
  if(mode==1) r=fmaxf(r,0.f);
  if(mode==2) r=rbf(r+bf2f(R[m*N+n]));
  C[m*N+n]=f2bf(r);
}
__global__ void attn_k(const u16* qkv,u16* o){
  int i=blockIdx.x; int h=blockIdx.y; int t=threadIdx.x;
  __shared__ float sc[256]; __shared__ float red[4];
  int j=i-255+t;
  float s=-1e9f; bool valid = j>=0;
  if(valid){ float a=0; for(int d=0;d<DH;d++) a+=bf2f(qkv[i*1920+h*DH+d])*bf2f(qkv[j*1920+640+h*DH+d]); s=rbf(rbf(a)*rbf(0.22360679774997896f)); }
  float m=s; for(int of=32;of>0;of>>=1) m=fmaxf(m,__shfl_xor(m,of));
  if((t&63)==0) red[t>>6]=m; __syncthreads();
  m=fmaxf(fmaxf(red[0],red[1]),fmaxf(red[2],red[3])); __syncthreads();
  float p= valid? __expf(s-m):0.f; float l=p; for(int of=32;of>0;of>>=1) l+=__shfl_xor(l,of);
  if((t&63)==0) red[t>>6]=l; __syncthreads();
  l=red[0]+red[1]+red[2]+red[3];
  sc[t]=rbf(p/l); __syncthreads();
  if(t<DH){ float a=0; for(int jj=0;jj<256;jj++){ int j2=i-255+jj; if(j2>=0) a+=sc[jj]*bf2f(qkv[j2*1920+1280+h*DH+t]); } o[i*D+h*DH+t]=f2bf(a);}
}
extern "C" void kernel_launch(void* const* d_in, const int* in_sizes, int n_in,
                              void* d_out, int out_size, void* d_ws, size_t ws_size,
                              hipStream_t stream) {
  const u16* x=(const u16*)d_in[0]; const u16* wqkv=(const u16*)d_in[1]; const u16* wo=(const u16*)d_in[2];
  const u16* w1=(const u16*)d_in[4]; const u16* w2=(const u16*)d_in[5];
  u16* ws=(u16*)d_ws;
  u16* xn=ws; u16* qkv=xn+S*D; u16* o=qkv+S*1920; u16* h=o+S*D; u16* hn=h+S*D; u16* f=hn+S*D;
  ln_k<<<S,64,0,stream>>>(x,xn);
  gemm_k<<<dim3(30,S),64,0,stream>>>(xn,wqkv,qkv,nullptr,S,1920,D,0);
  attn_k<<<dim3(S,H),256,0,stream>>>(qkv,o);
  gemm_k<<<dim3(10,S),64,0,stream>>>(o,wo,h,x,S,D,D,2);
  ln_k<<<S,64,0,stream>>>(h,hn);
  gemm_k<<<dim3(27,S),64,0,stream>>>(hn,w1,f,nullptr,S,DFF,D,1);
  gemm_k<<<dim3(10,S),64,0,stream>>>(f,w2,(u16*)d_out,h,S,D,DFF,2);
}
```

```cpp
#include <hip/hip_runtime.h>
#include <hip/hip_bf16.h>
#include <stdint.h>

typedef unsigned short u16;
typedef unsigned int u32;
typedef unsigned long long u64;
typedef u32 v4u __attribute__((ext_vector_type(4)));
typedef u32 v2u __attribute__((ext_vector_type(2)));
typedef float f32x4 __attribute__((ext_vector_type(4)));
typedef float f32x2 __attribute__((ext_vector_type(2)));
typedef float f32x16 __attribute__((ext_vector_type(16)));
typedef short s16x4 __attribute__((ext_vector_type(4)));
typedef __bf16 bf16x8 __attribute__((ext_vector_type(8)));
typedef __bf16 bf16x2 __attribute__((ext_vector_type(2)));
#define LAS __attribute__((address_space(3)))
#define GAS __attribute__((address_space(1)))
#define DI __device__ __forceinline__
#define RLX_AGENT __ATOMIC_RELAXED, __HIP_MEMORY_SCOPE_AGENT

#define STAMPS 0

namespace fb {
constexpr int WS_EPOCH = 0;
constexpr int WS_FLAGS = 4096;
constexpr int WS_IMG = 16384;
constexpr int WS_OF = WS_IMG + 3 * 128 * 20480;
constexpr int WS_HF = WS_OF + 16 * 8 * 20 * 1024;
constexpr int WS_ST = WS_HF + 16 * 8 * 20 * 1024;
constexpr int WS_FF = WS_ST + 2048 * 64;
constexpr int WS_WOT = WS_FF + 16 * 8 * 54 * 1024;
constexpr int WS_W1T = WS_WOT + 20 * 40 * 1024;
constexpr int WS_W2T = WS_W1T + 20 * 108 * 1024;
constexpr int WS_CS1 = WS_W2T + 54 * 40 * 1024;
constexpr int WS_END = WS_CS1 + 20 * 1792 * 4;
constexpr int WS_STAMP = WS_END;
constexpr int LDS_BYTES = 163840;
constexpr int L_RING = 0;
constexpr int L_UNIT = 147456;
constexpr int L_TAB = 162816;
constexpr int L_CS = L_TAB, L_PROG = L_TAB + 512;
constexpr int L_WO = 0;
constexpr int L_K = 92160;
constexpr int L_V = L_K + 24 * 1280, V_TS = 1616;
constexpr int L_W1A = 102400;
constexpr int L_W1B = 0;
constexpr int L_W2 = 0;
constexpr int AUX_SC1 = 16;
constexpr u32 SPIN_CAP = 1u << 17;

DI f32x4 mfma(v4u a, v4u b, f32x4 c) {
  return __builtin_amdgcn_mfma_f32_16x16x32_bf16(__builtin_bit_cast(bf16x8, a), __builtin_bit_cast(bf16x8, b), c, 0, 0, 0);
}
DI f32x16 mfma32(v4u a, v4u b, f32x16 c) {
  return __builtin_amdgcn_mfma_f32_32x32x16_bf16(__builtin_bit_cast(bf16x8, a), __builtin_bit_cast(bf16x8, b), c, 0, 0, 0);
}
DI float max3(float a, float b, float c) { float r; asm("v_max3_f32 %0, %1, %2, %3" : "=v"(r) : "v"(a), "v"(b), "v"(c)); return r; }
DI v2u trr(LAS char* p) { return __builtin_bit_cast(v2u, __builtin_amdgcn_ds_read_tr16_b64_v4i16((LAS s16x4*)p)); }
DI v4u trfrag(LAS char* p0, LAS char* p1) { v2u a = trr(p0), b = trr(p1); return v4u{a.x, a.y, b.x, b.y}; }
DI u32 pk2(float a, float b) { f32x2 v = {a, b}; return __builtin_bit_cast(u32, __builtin_convertvector(v, bf16x2)); }
DI float bflo(u32 u) { return __uint_as_float(u << 16); }
DI float bfhi(u32 u) { return __uint_as_float(u & 0xFFFF0000u); }
DI float rbf(float f) { return bflo(pk2(f, 0.f)); }
DI float dot2(u32 a, u32 b, float c) {
  return __builtin_amdgcn_fdot2_f32_bf16(__builtin_bit_cast(bf16x2, a), __builtin_bit_cast(bf16x2, b), c, false);
}
DI float sum4(float v) {
  auto r = __builtin_amdgcn_permlane16_swap(__float_as_uint(v), __float_as_uint(v), false, false);
  v = __uint_as_float(r[0]) + __uint_as_float(r[1]);
  auto s = __builtin_amdgcn_permlane32_swap(__float_as_uint(v), __float_as_uint(v), false, false);
  return __uint_as_float(s[0]) + __uint_as_float(s[1]);
}
DI float max4(float v) {
  auto r = __builtin_amdgcn_permlane16_swap(__float_as_uint(v), __float_as_uint(v), false, false);
  v = fmaxf(__uint_as_float(r[0]), __uint_as_float(r[1]));
  auto s = __builtin_amdgcn_permlane32_swap(__float_as_uint(v), __float_as_uint(v), false, false);
  return fmaxf(__uint_as_float(s[0]), __uint_as_float(s[1]));
}
DI void dma16(const void* sbase, u32 voff, u32 lds_dst) {
  u32 keep;
  asm volatile("s_mov_b32 %0, m0\n\ts_mov_b32 m0, %3\n\ts_nop 0\n\tglobal_load_lds_dwordx4 %1, %2\n\ts_mov_b32 m0, %0"
               : "=&s"(keep) : "v"(voff), "s"(sbase), "s"(__builtin_amdgcn_readfirstlane(lds_dst)) : "memory");
}
#define VMWAIT(n) asm volatile("s_waitcnt vmcnt(" #n ")" ::: "memory")
#define BAR() do { asm volatile("s_waitcnt lgkmcnt(0)" ::: "memory"); __builtin_amdgcn_s_barrier(); asm volatile("" ::: "memory"); } while (0)
#define SCHED() __builtin_amdgcn_sched_barrier(0)
#define SGB(mask, n) __builtin_amdgcn_sched_group_barrier(mask, n, 0)
DI LAS char* opaque(LAS char* p) { asm volatile("" : "+v"(p)); return p; }

DI u64 mix64(u64 z) {
  z += 0x9E3779B97F4A7C15ull; z = (z ^ (z >> 30)) * 0xBF58476D1CE4E5B9ull; z = (z ^ (z >> 27)) * 0x94D049BB133111EBull;
  return z ^ (z >> 31);
}
DI void poll_flags(GAS u64* p, bool active, u64 tag) {
  for (u32 spins = 0; spins < SPIN_CAP; ++spins) {
    u64 v = tag;
    if (active) v = __hip_atomic_load(p, RLX_AGENT);
    if (__all(v == tag)) break;
    __builtin_amdgcn_s_sleep(1);
  }
  __builtin_amdgcn_fence(__ATOMIC_ACQUIRE, "wavefront");
}

DI void setp(LAS char* L, int wave, int lane, u32 step) {
  asm volatile("s_waitcnt lgkmcnt(0)" ::: "memory");
  if (lane == 0) *(volatile LAS u32*)(L + L_PROG + wave * 4) = step;
  asm volatile("" ::: "memory");
}
DI void waitp(LAS char* L, int grp, u32 step) {
  for (u32 spins = 0; spins < (1u << 24); ++spins) {
    const v4u x = *(volatile LAS v4u*)(L + L_PROG + grp * 16);
    const u32 m = min(min(x.x, x.y), min(x.z, x.w));
    if (__builtin_amdgcn_readfirstlane(m) >= step) break;
  }
  asm volatile("" ::: "memory");
}
DI void waitps(LAS char* L, int grp, u32 step) {
  for (u32 spins = 0; spins < (1u << 22); ++spins) {
    const v4u x = *(volatile LAS v4u*)(L + L_PROG + grp * 16);
    const u32 m = min(min(x.x, x.y), min(x.z, x.w));
    if (__builtin_amdgcn_readfirstlane(m) >= step) break;
    __builtin_amdgcn_s_sleep(2);
  }
  asm volatile("" ::: "memory");
}
DI void waitp1(LAS char* L, int w, u32 step) {
  for (u32 spins = 0; spins < (1u << 24); ++spins) {
    const u32 x = *(volatile LAS u32*)(L + L_PROG + w * 4);
    if (__builtin_amdgcn_readfirstlane(x) >= step) break;
  }
  asm volatile("" ::: "memory");
}

struct Params {
  const char* x; const char* wqkv; const char* wo; const char* w1; const char* w2; char* out; char* ws;
};

#define STAMP(k) do {} while (0)

__global__ void __launch_bounds__(512, 2) fused_block(Params P) {
  extern __shared__ __attribute__((aligned(16))) char smem[];
  LAS char* L = (LAS char*)smem;
  const u32 lds0 = (u32)(uintptr_t)L;
  const int tid = threadIdx.x, lane = tid & 63;
  const int wave = __builtin_amdgcn_readfirstlane(tid >> 6);
  const int bid = blockIdx.x;
  const int gi = (bid & 7) * 2 + (bid >> 7);
  const int gj = (bid >> 3) & 15;
  const int tok = lane & 15, g4 = lane >> 4, q4 = (lane & 15) >> 2, p4 = lane & 3;
  const bool isM = wave < 4;
  const int v = wave & 3;
  const __amdgpu_buffer_rsrc_t rs = __builtin_amdgcn_make_buffer_rsrc((void*)P.ws, 0, WS_END, 0x00020000);
  GAS u64* flags = (GAS u64*)(P.ws + WS_FLAGS);
  const int rot = (gi + gj) % 10;
  const int rh = gj >> 3, ct = gj & 7;
  const int T0 = gj < 12 ? 7 * gj : 84 + 6 * (gj - 12);
  const int nT = gj < 12 ? 7 : 6;
  const int r5 = (ct * 30) >> 3;
  const v4u ones = {0x3F803F80u, 0x3F803F80u, 0x3F803F80u, 0x3F803F80u};
  const f32x4 zero = {0.f, 0.f, 0.f, 0.f};
  const int l16 = lane * 16;
  STAMP(0);
  if (tid < 8) *(volatile LAS u32*)(L + L_PROG + tid * 4) = 0u;
  u64 tag = 0;
  if (wave == 0 || wave >= 4) tag = mix64(__hip_atomic_load((GAS u64*)(P.ws + WS_EPOCH), RLX_AGENT));

  if (gj == 15) {
    const int R = gi * 8 + wave;
    LAS char* stg = L + wave * 4096;
    const int srow = lane >> 3, sc = (lane & 7) * 16;
    LAS char* tb = stg + (4 * g4 + q4) * 128 + p4 * 8;
    v4u d[5][4];
    const __amdgpu_buffer_rsrc_t rwo = __builtin_amdgcn_make_buffer_rsrc((void*)P.wo, 0, 640 * 1280, 0x00020000);
    const __amdgpu_buffer_rsrc_t rw1 = __builtin_amdgcn_make_buffer_rsrc((void*)P.w1, 0, 640 * 3456, 0x00020000);
#pragma unroll
    for (int u = 0; u < 5; ++u) {
      const int U = R + 128 * u;
      if (u < 2 && U < 200) {
        const int ks = U / 10, lg = U - ks * 10;
#pragma unroll
        for (int e = 0; e < 4; ++e) {
          const int rho = 8 * e + srow, t = rho >> 4, gg = (rho >> 2) & 3, qq = rho & 3;
          const int row = ks < 16 ? (2 * ks + (gg >> 1)) * 20 + 8 * t + 4 * (gg & 1) + qq : (2 * (4 * (ks - 16) + gg) + t) * 20 + 16 + qq;
          d[u][e] = __builtin_amdgcn_raw_buffer_load_b128(rwo, row * 1280 + sc, lg * 128, 0);
        }
      } else {
        const int U1 = U < 560 ? U - 200 : 0, ks = U1 / 27, cg = U1 - ks * 27;
#pragma unroll
        for (int e = 0; e < 4; ++e) d[u][e] = __builtin_amdgcn_raw_buffer_load_b128(rw1, (8 * e + srow) * 3456 + sc, ks * (32 * 3456) + cg * 128, 0);
      }
    }
    SCHED();
    BAR();
#pragma unroll
    for (int u = 0; u < 5; ++u) {
      const int U = R + 128 * u;
#pragma unroll
      for (int e = 0; e < 4; ++e) *(LAS v4u*)(stg + e * 1024 + l16) = d[u][e];
      if (u < 2 && U < 200) {
        const int ks = U / 10, lg = U - ks * 10;
#pragma unroll
        for (int n = 0; n < 4; ++n)
          __builtin_amdgcn_raw_buffer_store_b128(trfrag(tb + n * 32, tb + n * 32 + 2048), rs, l16, WS_WOT + (ks * 40 + 4 * lg + n) * 1024, AUX_SC1);
      } else if (U < 560) {
        const int U1 = U - 200, ks = U1 / 27, cg = U1 - ks * 27;
#pragma unroll
        for (int n = 0; n < 4; ++n) {
          const v4u f = trfrag(tb + n * 32, tb + n * 32 + 2048);
          __builtin_amdgcn_raw_buffer_store_b128(f, rs, l16, WS_W1T + (ks * 108 + 4 * cg + n) * 1024, AUX_SC1);
          const f32x4 cs = mfma(f, ones, zero);
          if (tok == 0) __builtin_amdgcn_raw_buffer_store_b128(__builtin_bit_cast(v4u, cs), rs, g4 * 16, WS_CS1 + (ks * 1792 + cg * 64 + 16 * n) * 4, AUX_SC1);
        }
      }
      if (u == 1) {
        VMWAIT(0);
        BAR();
        if (tid == 0) __hip_atomic_store(flags + (4 * 16 + 0) * 16 + gi, tag, RLX_AGENT);
      }
    }
    VMWAIT(0);
    STAMP(1);
    BAR();
    if (tid == 0) __hip_atomic_store(flags + (4 * 16 + 1) * 16 + gi, tag, RLX_AGENT);
    if (isM) setp(L, wave, lane, 3);
  } else if (isM) {
    const __amdgpu_buffer_rsrc_t rx = __builtin_amdgcn_make_buffer_rsrc((void*)P.x, 0, 2048 * 1280, 0x00020000);
    const int xo = (128 * gi + 32 * wave + tok) * 1280 + g4 * 16;
    constexpr int W = 5;
    v4u a[W][4];
#pragma unroll
    for (int c = 0; c < W; ++c) {
      int kc = c + rot; kc = kc >= 10 ? kc - 10 : kc;
#pragma unroll
      for (int f = 0; f < 4; ++f) a[c][f] = __builtin_amdgcn_raw_buffer_load_b128(rx, xo + (f >> 1) * 64 + (f & 1) * (16 * 1280), kc * 128, 0);
    }
    SCHED();
    BAR();
    f32x4 acc1[2][8];
#pragma unroll
    for (int m = 0; m < 2; ++m)
#pragma unroll
      for (int n = 0; n < 8; ++n) acc1[m][n] = zero;
    f32x4 sx[2] = {zero, zero}, sq[2] = {zero, zero};
    LAS char* bn[8];
#pragma unroll
    for (int n = 0; n < 8; ++n) bn[n] = L + L_RING + (8 * g4 + q4) * 256 + ((n ^ (q4 | ((g4 & 1) << 2))) * 32) + p4 * 8;
    v4u b[2][8];
    waitp(L, 1, 1);
#pragma unroll
    for (int n = 0; n < 8; ++n) b[0][n] = trfrag(bn[n], bn[n] + 1024);
#pragma unroll
    for (int s = 0; s < 20; ++s) {
      const int c = s >> 1, ks = s & 1;
      if (s + 1 < 20) {
        if (ks == 1) waitp(L, 1, c + 2);
        if (s + 1 == 8 || s + 1 == 16) {
#pragma unroll
          for (int n = 0; n < 8; ++n) bn[n] = opaque(bn[n] + 65536);
        }
        if (s + 1 == 18) {
#pragma unroll
          for (int n = 0; n < 8; ++n) bn[n] = opaque(bn[n] - 131072);
        }
        const int so = ((s + 1 < 18 ? s + 1 : s + 1 - 18) * 8192) & 0xFFFF;
#pragma unroll
        for (int n = 0; n < 8; ++n) b[(s + 1) & 1][n] = trfrag(bn[n] + so, bn[n] + so + 1024);
      }
      const v4u a0 = a[c % W][2 * ks], a1 = a[c % W][2 * ks + 1];
#pragma unroll
      for (int n = 0; n < 8; ++n) {
        acc1[0][n] = mfma(b[s & 1][n], a0, acc1[0][n]);
        acc1[1][n] = mfma(b[s & 1][n], a1, acc1[1][n]);
      }
      sx[0] = mfma(ones, a0, sx[0]); sx[1] = mfma(ones, a1, sx[1]);
      sq[0] = mfma(a0, a0, sq[0]); sq[1] = mfma(a1, a1, sq[1]);
      if (ks == 1 && c + W < 10) {
        int kn = W + c + rot; kn = kn >= 10 ? kn - 10 : kn;
#pragma unroll
        for (int f = 0; f < 4; ++f) a[c % W][f] = __builtin_amdgcn_raw_buffer_load_b128(rx, xo + (f >> 1) * 64 + (f & 1) * (16 * 1280), kn * 128, 0);
      }
#pragma unroll
      for (int n = 0; n < 16; ++n) { SGB(0x8, 1); SGB(0x100, 1); }
      SCHED();
      if (s == 1) setp(L, wave, lane, 1);
    }
    float mb[2], rstd[2];
    const int sec = gj / 5, jj = gj - 5 * sec;
    const float qs = sec == 0 ? 0.22360679774997896f * 1.4426950408889634f : 1.f;
#pragma unroll
    for (int m = 0; m < 2; ++m) {
      const float mean = sx[m][0] * (1.f / 640.f);
      const float dg = p4 == 0 ? sq[m][0] : (p4 == 1 ? sq[m][1] : (p4 == 2 ? sq[m][2] : sq[m][3]));
      const float ssq = sum4(g4 == q4 ? dg : 0.f);
      const float var = fmaxf(ssq * (1.f / 640.f) - mean * mean, 0.f);
      mb[m] = rbf(mean);
      rstd[m] = qs / rbf(sqrtf(rbf(rbf(var) + 1e-5f)));
    }
    STAMP(1);
    setp(L, wave, lane, 2);
    waitp(L, 1, 11);
    const int so = WS_IMG + ((sec * 128 + 8 * gi + 2 * wave) * 80 + 16 * jj) * 256;
    const int vo = (2 * (g4 & 1) + (g4 >> 1)) * 256 + tok * 16;
#pragma unroll
    for (int e = 0; e < 4; ++e) {
      const f32x4 c0 = *(LAS f32x4*)(L + L_CS + (32 * e + 4 * g4) * 4), c1 = *(LAS f32x4*)(L + L_CS + (32 * e + 16 + 4 * g4) * 4);
#pragma unroll
      for (int m = 0; m < 2; ++m) {
        const f32x4 x = acc1[m][2 * e], y = acc1[m][2 * e + 1];
        const u32 x0 = pk2((x[0] - mb[m] * c0[0]) * rstd[m], (x[1] - mb[m] * c0[1]) * rstd[m]);
        const u32 x1 = pk2((x[2] - mb[m] * c0[2]) * rstd[m], (x[3] - mb[m] * c0[3]) * rstd[m]);
        const u32 y0 = pk2((y[0] - mb[m] * c1[0]) * rstd[m], (y[1] - mb[m] * c1[1]) * rstd[m]);
        const u32 y1 = pk2((y[2] - mb[m] * c1[2]) * rstd[m], (y[3] - mb[m] * c1[3]) * rstd[m]);
        auto r0 = __builtin_amdgcn_permlane16_swap(x0, y0, false, false);
        auto r1 = __builtin_amdgcn_permlane16_swap(x1, y1, false, false);
        __builtin_amdgcn_raw_buffer_store_b128(v4u{r0[0], r1[0], r0[1], r1[1]}, rs, vo, so + m * 20480 + e * 1024, AUX_SC1);
      }
    }
    STAMP(14);
    VMWAIT(0);
    setp(L, wave, lane, 3);
  } else {
    const int U = (gi * 15 + gj) * 3 + v;
    const bool hasU = v < 3;
    const bool isW2 = U < 540;
    const int uks = isW2 ? U / 10 : (U - 180) / 27, ulg = isW2 ? U - uks * 10 : (U - 180) - uks * 27;
    if (hasU) {
      const char* src = isW2 ? P.w2 + (size_t)uks * (32 * 1280) + ulg * 128 : P.w1 + (size_t)uks * (32 * 3456) + ulg * 128;
      const int rstr = isW2 ? 1280 : 3456;
#pragma unroll
      for (int e = 0; e < 4; ++e) dma16(src, (8 * e + (lane >> 3)) * rstr + (lane & 7) * 16, lds0 + L_UNIT + v * 4096 + e * 1024);
    }
    u32 so[2];
#pragma unroll
    for (int k = 0; k < 2; ++k) {
      const int f = g4 | (k << 2);
      so[k] = g4 * 3840 + (((tok >> 1) ^ f) * 32) + (tok & 1) * 16;
    }
    const char* wsrc = P.wqkv + gj * 256 + (size_t)(16 * v) * 3840;
#define P1_ISSUE(c) { int kc = (c) + rot; kc = kc >= 10 ? kc - 10 : kc; const char* src = wsrc + (size_t)kc * (64 * 3840); \
      _Pragma("unroll") for (int e = 0; e < 4; ++e) dma16(src + e * (4 * 3840), so[e >> 1], lds0 + L_RING + ((c) < 9 ? (c) : 0) * 16384 + (4 * v + e) * 1024); }
    P1_ISSUE(0) P1_ISSUE(1) P1_ISSUE(2)
    BAR();
    LAS char* cb0 = L + L_RING + (8 * g4 + q4) * 256 + (((2 * v) ^ (q4 | ((g4 & 1) << 2))) * 32) + p4 * 8;
    LAS char* cb1 = L + L_RING + (8 * g4 + q4) * 256 + (((2 * v + 1) ^ (q4 | ((g4 & 1) << 2))) * 32) + p4 * 8;
    f32x4 cs[2] = {zero, zero};
#define P1_CS(c) { waitp(L, 1, (c) + 1); const int ro = ((c) < 9 ? (c) : 0) * 16384; \
      _Pragma("unroll") for (int ks = 0; ks < 2; ++ks) { \
        cs[0] = mfma(trfrag(cb0 + ro + ks * 8192, cb0 + ro + ks * 8192 + 1024), ones, cs[0]); \
        cs[1] = mfma(trfrag(cb1 + ro + ks * 8192, cb1 + ro + ks * 8192 + 1024), ones, cs[1]); } }
#define VMW2(a, b) { if (hasU) VMWAIT(a); else VMWAIT(b); }
    VMWAIT(8); setp(L, wave, lane, 1); P1_ISSUE(3) P1_ISSUE(4)
    VMWAIT(12); setp(L, wave, lane, 2); P1_ISSUE(5) P1_ISSUE(6) P1_CS(0)
    VMWAIT(16); setp(L, wave, lane, 3); P1_ISSUE(7) P1_ISSUE(8) P1_CS(1)
    VMWAIT(20); setp(L, wave, lane, 4);
    waitp(L, 0, 1); waitp(L, 1, 3); P1_ISSUE(9)
    if (hasU) {
      LAS char* tb = L + L_UNIT + v * 4096 + (4 * g4 + q4) * 128 + p4 * 8;
      const int dst = isW2 ? WS_W2T + (uks * 40 + 4 * ulg) * 1024 : WS_W1T + (uks * 108 + 4 * ulg) * 1024;
      f32x4 c4[4];
#pragma unroll
      for (int n = 0; n < 4; ++n) {
        const v4u f = trfrag(tb + n * 32, tb + n * 32 + 2048);
        __builtin_amdgcn_raw_buffer_store_b128(f, rs, l16, dst + n * 1024, AUX_SC1);
        c4[n] = mfma(f, ones, zero);
      }
      const f32x4 cv = tok == 0 ? c4[0] : (tok == 1 ? c4[1] : (tok == 2 ? c4[2] : c4[3]));
      __builtin_amdgcn_raw_buffer_store_b128(__builtin_bit_cast(v4u, cv), rs, (tok < 4 && !isW2) ? (tok * 64 + g4 * 16) : 0x7FFFFFF0 - WS_END,
                                             WS_CS1 + (uks * 1792 + ulg * 64) * 4, AUX_SC1);
    }
    P1_CS(2)
    VMW2(25, 20) setp(L, wave, lane, 5); P1_CS(3)
    VMW2(21, 16) setp(L, wave, lane, 6); P1_CS(4)
    VMW2(17, 12) setp(L, wave, lane, 7); P1_CS(5)
    VMW2(13, 8) setp(L, wave, lane, 8); P1_CS(6)
    VMW2(9, 4) setp(L, wave, lane, 9); P1_CS(7)
    VMW2(5, 0) setp(L, wave, lane, 10); P1_CS(8)
    P1_CS(9)
    if (tok == 0) {
      *(LAS f32x4*)(L + L_CS + (16 * (2 * v) + 4 * g4) * 4) = cs[0];
      *(LAS f32x4*)(L + L_CS + (16 * (2 * v + 1) + 4 * g4) * 4) = cs[1];
    }
    VMWAIT(0);
    setp(L, wave, lane, 11);
    STAMP(1);
  }
  STAMP(2);
  const int hh = wave & 1, qg = wave >> 1;
  v4u pre[21];
  u64 fl[5];
  if (isM) {
    if (wave == 0) {
      waitp(L, 0, 3);
      if (gj != 15) waitp(L, 1, 11);
      if (lane == 0) __hip_atomic_store(flags + (0 * 16 + gi) * 16 + gj, tag, RLX_AGENT);
      const int slot = lane >> 1, sc = slot == 0 ? 0 : (slot < 4 ? 1 : 2);
      const int row = slot == 0 ? gi : gi - 2 + (slot < 4 ? slot - 1 : slot - 4);
      const int jn = 5 * sc + ((10 * gj + 9 * (lane & 1)) >> 5);
      const bool act = lane < 14 && row >= 0;
      poll_flags(flags + (0 * 16 + (act ? row : 0)) * 16 + (act ? jn : 0), act, tag);
      setp(L, wave, lane, 4);
    } else waitp1(L, 0, 4);
    STAMP(3);
  } else {
    if (wave == 4) {
      poll_flags(flags + (4 * 16 + 0) * 16 + (lane & 15), lane < 16, tag);
      __builtin_amdgcn_fence(__ATOMIC_ACQUIRE, "agent");
      VMWAIT(0);
      setp(L, wave, lane, 12);
    }
    waitp1(L, 0, 4);
  }
  v4u q0, q1;
  {
    const int r32 = lane & 31;
    const int qo = ((8 * gi + 2 * qg + (r32 >> 4)) * 80 + 5 * gj) * 256 + (r32 & 15) * 16;
    q0 = __builtin_amdgcn_raw_buffer_load_b128(rs, qo + (3 * hh + (lane >> 5)) * 256, WS_IMG, AUX_SC1);
    q1 = __builtin_amdgcn_raw_buffer_load_b128(rs, qo + 512, WS_IMG, AUX_SC1);
  }
  if (isM) {
    const int isV = wave >> 1, t0 = 12 * (wave & 1);
    const int gb = WS_IMG + (((1 + isV) * 128 + 8 * gi - 16 + t0) * 80 + 5 * gj) * 256;
    const int gmin = 16 - 8 * gi - t0;
    int go[5], lo[5];
#pragma unroll
    for (int e = 0; e < 5; ++e) {
      const int x = e * 64 + lane, t4 = x / 80, rem = x - t4 * 80;
      go[e] = t4 * 20480 + rem * 16;
      lo[e] = isV ? L_V + (t0 + t4) * V_TS + (rem >> 4) * 320 + tok * 16 : L_K + t0 * 1280 + x * 16;
    }
    v4u hv[3][5];
#pragma unroll
    for (int g = 0; g < 3; ++g)
      if (4 * g >= gmin) {
#pragma unroll
        for (int e = 0; e < 5; ++e) hv[g][e] = __builtin_amdgcn_raw_buffer_load_b128(rs, go[e], gb + g * (4 * 20480), AUX_SC1);
      }
    SCHED();
    setp(L, wave, lane, 5);
    STAMP(16);
    if (wave == 2 && lane < 24) *(LAS v4u*)(L + L_V + lane * V_TS + 1600) = ones;
    const int lstr = isV ? 4 * V_TS : 4 * 1280;
#pragma unroll
    for (int g = 0; g < 3; ++g)
      if (4 * g >= gmin) {
#pragma unroll
        for (int e = 0; e < 5; ++e) *(LAS v4u*)(L + lo[e] + g * lstr) = hv[g][e];
      }
  } else {
    waitp(L, 0, 5);
    waitp1(L, 4, 12);
    STAMP(15);
    const char* src = P.ws + WS_WOT + ct * 5120;
#pragma unroll 1
    for (int u = v; u < 90; u += 4) { const int ks = u / 5; dma16(src + ks * 40960 + (u - 5 * ks) * 1024, l16, lds0 + L_WO + u * 1024); }
#pragma unroll
    for (int r = 0; r < 4; ++r) fl[r] = __hip_atomic_load(flags + r * 64 + lane, RLX_AGENT);
    fl[4] = __hip_atomic_load(flags + (4 * 16 + 1) * 16 + (lane & 15), RLX_AGENT);
    STAMP(3);
  }
  if (lane >= 32) q1 = v4u{0u, 0u, 0u, 0u};
  else if (hh == 0) { q1.z = 0u; q1.w = 0u; }
  else { q1.x = 0u; q1.y = 0u; }
  BAR();
  STAMP(4);
  {
    const int r32 = lane & 31, h = lane >> 5, dg = (lane >> 4) & 1;
    const float NEG = -__builtin_inff();
    LAS char* k1p = L + L_K + (2 * qg + (r32 >> 4)) * 1280 + (r32 & 15) * 16;
    LAS char* k0p = k1p + (3 * hh + h) * 256;
    k1p += 512;
    LAS char* vp[2];
#pragma unroll
    for (int t = 0; t < 2; ++t) {
      const int key = 8 * t + 4 * h + q4, gam = 5 * hh + (dg ? 4 : p4);
      const int off = (dg && p4) ? 1600 : (gam >> 1) * 320 + key * 16 + (gam & 1) * 8;
      vp[t] = L + L_V + (2 * qg) * V_TS + off;
    }
    f32x16 o, z16;
#pragma unroll
    for (int i = 0; i < 16; ++i) { o[i] = 0.f; z16[i] = 0.f; }
    const int dq = r32 - 4 * h;
#define UP(i) (((i) & 3) + 8 * ((i) >> 2) > dq)
#define QK(b, c) mfma32(*(LAS v4u*)(k1p + (b) * 2560), q1, mfma32(*(LAS v4u*)(k0p + (b) * 2560), q0, c))
#define MASK_EDGE(s) _Pragma("unroll") for (int i = 0; i < 16; ++i) s[i] = UP(i) ? s[i] : NEG
#define MASK_DIAG(s) _Pragma("unroll") for (int i = 0; i < 16; ++i) s[i] = UP(i) ? NEG : s[i]
#define MAXS(s) _Pragma("unroll") for (int i = 0; i < 16; i += 4) { m = max3(m, s[i], s[i + 1]); m2 = max3(m2, s[i + 2], s[i + 3]); }
#define EX(x) __builtin_amdgcn_exp2f(x)
#define PV(b, s) { const v4u pa = {pk2(EX(s[0]), EX(s[1])), pk2(EX(s[2]), EX(s[3])), pk2(EX(s[4]), EX(s[5])), pk2(EX(s[6]), EX(s[7]))}; \
      const v4u pb = {pk2(EX(s[8]), EX(s[9])), pk2(EX(s[10]), EX(s[11])), pk2(EX(s[12]), EX(s[13])), pk2(EX(s[14]), EX(s[15]))}; \
      o = mfma32(trfrag(vp[0] + (b) * (2 * V_TS), vp[1] + (b) * (2 * V_TS)), pa, o); o = mfma32(trfrag(vp[0] + (b) * (2 * V_TS) + V_TS, vp[1] + (b) * (2 * V_TS) + V_TS), pb, o); }
    float m = NEG, m2 = NEG;
    const int bmin = 8 - 4 * gi - qg;
    if (bmin <= 0) {
#pragma unroll
      for (int b = 0; b < 9; ++b) {
        f32x16 s = QK(b, z16);
        if (b == 0) MASK_EDGE(s);
        if (b == 8) MASK_DIAG(s);
        MAXS(s)
      }
    } else {
      { f32x16 s = QK(8, z16); MASK_DIAG(s); MAXS(s) }
#pragma unroll 1
      for (int b = bmin; b < 8; ++b) { f32x16 s = QK(b, z16); MAXS(s) }
    }
    m = max3(m, m2, m2);
    { auto r = __builtin_amdgcn_permlane32_swap(__float_as_uint(m), __float_as_uint(m), false, false); m = max3(m, __uint_as_float(r[0]), __uint_as_float(r[1])); }
    if (!isM) {
      if (!__all(fl[0] == tag && fl[1] == tag && fl[2] == tag && fl[3] == tag && fl[4] == tag)) {
        poll_flags(flags + (4 * 16 + 1) * 16 + (lane & 15), true, tag);
#pragma unroll 1
        for (int r = 0; r < 4; ++r) poll_flags(flags + r * 64 + lane, true, tag);
      }
      __builtin_amdgcn_fence(__ATOMIC_ACQUIRE, "wavefront");
      if (bid == 0 && tid == 256) __hip_atomic_store((GAS u64*)(P.ws + WS_EPOCH), tag, RLX_AGENT);
#pragma unroll
      for (int e = 0; e < 21; ++e) {
        const int u = v + 4 * e, ks = u / 7;
        pre[e] = __builtin_amdgcn_raw_buffer_load_b128(rs, l16, WS_W1T + ((8 + ks) * 108 + T0 + (u - 7 * ks)) * 1024, AUX_SC1);
      }
      SCHED();
    }
    f32x16 nm;
#pragma unroll
    for (int i = 0; i < 16; ++i) nm[i] = -m;
    if (bmin <= 0) {
      f32x16 s = QK(0, nm);
#pragma unroll
      for (int b = 0; b < 9; ++b) {
        f32x16 sn = s;
        if (b < 8) sn = QK(b + 1, nm);
        if (b == 0) MASK_EDGE(s);
        if (b == 8) MASK_DIAG(s);
        PV(b, s)
        s = sn;
      }
    } else {
      { f32x16 s = QK(8, nm); MASK_DIAG(s); PV(8, s) }
#pragma unroll 1
      for (int b = bmin; b < 8; ++b) { f32x16 s = QK(b, nm); PV(b, s) }
    }
    const float inv = __builtin_amdgcn_rcpf(o[12]);
    const int mt = 2 * qg + (r32 >> 4);
    const int ob = WS_OF + (gi * 8) * 20 * 1024;
    __builtin_amdgcn_raw_buffer_store_b128(
        v4u{pk2(o[0] * inv, o[1] * inv), pk2(o[2] * inv, o[3] * inv), pk2(o[4] * inv, o[5] * inv), pk2(o[6] * inv, o[7] * inv)}, rs,
        mt * 20480 + ((2 * hh + h) * 16 + tok) * 16, ob + gj * 1024, AUX_SC1);
    if (h == 0)
      __builtin_amdgcn_raw_buffer_store_b64(v2u{pk2(o[8] * inv, o[9] * inv), pk2(o[10] * inv, o[11] * inv)}, rs,
          mt * 20480 + tok * 16, ob + (16 + (gj >> 2)) * 1024 + (gj & 3) * 256 + 8 * hh, AUX_SC1);
  }
  VMWAIT(0);
  BAR();
  STAMP(5);
  const int mt3 = 4 * rh + v;
  if (isM) {
    u32 hres[10];
    if (wave == 0 && lane == 0) __hip_atomic_store(flags + (1 * 16 + gi) * 16 + gj, tag, RLX_AGENT);
    v2u xres[5];
    const GAS char* xr = (const GAS char*)P.x + (size_t)(128 * gi + 16 * mt3 + tok) * 1280 + ct * 160 + g4 * 8;
#pragma unroll
    for (int t = 0; t < 5; ++t) xres[t] = *(const GAS v2u*)(xr + t * 32);
    if (wave == 0) { poll_flags(flags + (1 * 16 + gi) * 16 + (lane & 15), lane < 16, tag); setp(L, wave, lane, 7); }
    else waitp1(L, 0, 7);
    STAMP(6);
    {
      v4u a[20];
      const int ab = WS_OF + (gi * 8 + mt3) * 20 * 1024;
      const int r3 = 2 * ct;
#pragma unroll
      for (int s = 0; s < 20; ++s) { const int ks = s < 16 ? ((s + r3) & 15) : s; a[s] = __builtin_amdgcn_raw_buffer_load_b128(rs, l16, ab + ks * 1024, AUX_SC1); }
      SCHED();
      setp(L, wave, lane, 8);
      STAMP(17);
      f32x4 acc[5];
#pragma unroll
      for (int n = 0; n < 5; ++n) acc[n] = zero;
      LAS char* bb = L + L_WO + l16;
      v4u bf[2][5];
      {
        LAS char* b = bb + r3 * 5120;
#pragma unroll
        for (int n = 0; n < 5; ++n) bf[0][n] = *(LAS v4u*)(b + n * 1024);
      }
#pragma unroll
      for (int s = 0; s < 20; ++s) {
        if (s == 17) waitp(L, 1, 13);
        if (s + 1 < 20) {
          const int ks = s + 1 < 16 ? ((s + 1 + r3) & 15) : s + 1;
          LAS char* b = bb + ks * 5120;
#pragma unroll
          for (int n = 0; n < 5; ++n) bf[(s + 1) & 1][n] = *(LAS v4u*)(b + n * 1024);
        }
#pragma unroll
        for (int n = 0; n < 5; ++n) acc[n] = mfma(bf[s & 1][n], a[s], acc[n]);
#pragma unroll
        for (int n = 0; n < 5; ++n) { SGB(0x8, 1); SGB(0x100, 1); }
        SCHED();
      }
      setp(L, wave, lane, 9);
      STAMP(18);
      float s1 = 0.f, s2 = 0.f;
#pragma unroll
      for (int n = 0; n < 5; ++n) {
        const float h0 = bflo(xres[n].x) + rbf(acc[n][0]), h1 = bfhi(xres[n].x) + rbf(acc[n][1]);
        const float h2 = bflo(xres[n].y) + rbf(acc[n][2]), h3 = bfhi(xres[n].y) + rbf(acc[n][3]);
        hres[2 * n] = pk2(h0, h1); hres[2 * n + 1] = pk2(h2, h3);
        s1 = dot2(hres[2 * n], 0x3F803F80u, s1); s1 = dot2(hres[2 * n + 1], 0x3F803F80u, s1);
        s2 = dot2(hres[2 * n], hres[2 * n], s2); s2 = dot2(hres[2 * n + 1], hres[2 * n + 1], s2);
      }
      const int hb = WS_HF + (gi * 8 + mt3) * 20 * 1024 + ((5 * ct) >> 1) * 1024;
      if ((ct & 1) == 0) {
        __builtin_amdgcn_raw_buffer_store_b128(v4u{hres[0], hres[1], hres[2], hres[3]}, rs, l16, hb, AUX_SC1);
        __builtin_amdgcn_raw_buffer_store_b128(v4u{hres[4], hres[5], hres[6], hres[7]}, rs, l16, hb + 1024, AUX_SC1);
        __builtin_amdgcn_raw_buffer_store_b64(v2u{hres[8], hres[9]}, rs, l16, hb + 2048, AUX_SC1);
      } else {
        __builtin_amdgcn_raw_buffer_store_b64(v2u{hres[0], hres[1]}, rs, l16 + 8, hb, AUX_SC1);
        __builtin_amdgcn_raw_buffer_store_b128(v4u{hres[2], hres[3], hres[4], hres[5]}, rs, l16, hb + 1024, AUX_SC1);
        __builtin_amdgcn_raw_buffer_store_b128(v4u{hres[6], hres[7], hres[8], hres[9]}, rs, l16, hb + 2048, AUX_SC1);
      }
      s1 = sum4(s1); s2 = sum4(s2);
      if (g4 == 0)
        __builtin_amdgcn_raw_buffer_store_b64(v2u{__float_as_uint(s1), __float_as_uint(s2)}, rs,
            WS_ST + (128 * gi + 16 * mt3 + tok) * 64 + ct * 8, 0, AUX_SC1);
      setp(L, wave, lane, 10);
      VMWAIT(0);
      setp(L, wave, lane, 11);
    }
    STAMP(7);
    if (wave == 0) {
      waitp(L, 0, 11);
      if (lane == 0) __hip_atomic_store(flags + (2 * 16 + gi) * 16 + gj, tag, RLX_AGENT);
      poll_flags(flags + (2 * 16 + gi) * 16 + (lane & 15), lane < 16, tag);
      setp(L, wave, lane, 12);
    } else waitp1(L, 0, 12);
    STAMP(8);
    {
      constexpr int W = 10;
      v4u a[W][2];
      const int ab = WS_HF + (gi * 8 + 2 * wave) * 20 * 1024;
      const int r4 = (gj * 5) >> 2;
#define KS4(s) ((s) + r4 >= 20 ? (s) + r4 - 20 : (s) + r4)
#pragma unroll
      for (int s = 0; s < W; ++s) {
        a[s][0] = __builtin_amdgcn_raw_buffer_load_b128(rs, l16, ab + KS4(s) * 1024, AUX_SC1);
        a[s][1] = __builtin_amdgcn_raw_buffer_load_b128(rs, l16, ab + (20 + KS4(s)) * 1024, AUX_SC1);
      }
      v4u stv[2];
#pragma unroll
      for (int m = 0; m < 2; ++m)
        stv[m] = __builtin_amdgcn_raw_buffer_load_b128(rs, WS_ST + (128 * gi + 32 * wave + 16 * m + tok) * 64 + g4 * 16, 0, AUX_SC1);
      SCHED();
      f32x4 acc[2][7];
#pragma unroll
      for (int m = 0; m < 2; ++m)
#pragma unroll
        for (int n = 0; n < 7; ++n) acc[m][n] = zero;
      LAS char* bb = L + l16;
#define W1OFF(ks) ((ks) < 8 ? L_W1A + (ks) * 7168 : L_W1B + ((ks) - 8) * 7168)
      waitp(L, 1, 15);
      STAMP(19);
      v4u bf[2][7];
      {
        LAS char* b = bb + W1OFF(r4);
#pragma unroll
        for (int n = 0; n < 7; ++n) bf[0][n] = *(LAS v4u*)(b + n * 1024);
      }
#pragma unroll
      for (int s = 0; s < 20; ++s) {
        if (s + 1 < 20) {
          const int ks = KS4(s + 1);
          LAS char* b = bb + W1OFF(ks);
#pragma unroll
          for (int n = 0; n < 7; ++n) bf[(s + 1) & 1][n] = *(LAS v4u*)(b + n * 1024);
        }
        const v4u a0 = a[s % W][0], a1 = a[s % W][1];
        if (s + W < 20) {
          a[s % W][0] = __builtin_amdgcn_raw_buffer_load_b128(rs, l16, ab + KS4(s + W) * 1024, AUX_SC1);
          a[s % W][1] = __builtin_amdgcn_raw_buffer_load_b128(rs, l16, ab + (20 + KS4(s + W)) * 1024, AUX_SC1);
        }
#pragma unroll
        for (int n = 0; n < 7; ++n) {
          acc[0][n] = mfma(bf[s & 1][n], a0, acc[0][n]);
          acc[1][n] = mfma(bf[s & 1][n], a1, acc[1][n]);
        }
#pragma unroll
        for (int n = 0; n < 7; ++n) { SGB(0x8, 2); SGB(0x100, 1); }
        SCHED();
        if (s == 9) { if (lane == 0) *(volatile LAS u32*)(L + L_PROG + wave * 4) = 13u; }
      }
      setp(L, wave, lane, 14);
      STAMP(12);
      float mb2[2], rs2[2];
#pragma unroll
      for (int m = 0; m < 2; ++m) {
        const float t1 = sum4(__uint_as_float(stv[m].x) + __uint_as_float(stv[m].z));
        const float t2 = sum4(__uint_as_float(stv[m].y) + __uint_as_float(stv[m].w));
        const float mean = t1 * (1.f / 640.f);
        const float var = fmaxf(t2 * (1.f / 640.f) - mean * mean, 0.f);
        mb2[m] = rbf(mean);
        rs2[m] = 1.f / rbf(sqrtf(rbf(rbf(var) + 1e-5f)));
      }
      u32 hp[2][7][2];
#pragma unroll
      for (int n = 0; n < 7; ++n) {
        const f32x4 cs = *(LAS f32x4*)(L + L_CS + (16 * n + 4 * g4) * 4);
#pragma unroll
        for (int m = 0; m < 2; ++m) {
          hp[m][n][0] = pk2(fmaxf((acc[m][n][0] - mb2[m] * cs[0]) * rs2[m], 0.f), fmaxf((acc[m][n][1] - mb2[m] * cs[1]) * rs2[m], 0.f));
          hp[m][n][1] = pk2(fmaxf((acc[m][n][2] - mb2[m] * cs[2]) * rs2[m], 0.f), fmaxf((acc[m][n][3] - mb2[m] * cs[3]) * rs2[m], 0.f));
        }
      }
      const int fbs = WS_FF + ((gi * 8 + 2 * wave) * 54 + (T0 >> 1)) * 1024;
#pragma unroll
      for (int m = 0; m < 2; ++m) {
        const int fb = fbs + m * 54 * 1024;
        if ((T0 & 1) == 0) {
#pragma unroll
          for (int e = 0; e < 3; ++e)
            __builtin_amdgcn_raw_buffer_store_b128(v4u{hp[m][2 * e][0], hp[m][2 * e][1], hp[m][2 * e + 1][0], hp[m][2 * e + 1][1]}, rs, l16, fb + e * 1024, AUX_SC1);
          if (nT == 7) __builtin_amdgcn_raw_buffer_store_b64(v2u{hp[m][6][0], hp[m][6][1]}, rs, l16, fb + 3 * 1024, AUX_SC1);
        } else {
          __builtin_amdgcn_raw_buffer_store_b64(v2u{hp[m][0][0], hp[m][0][1]}, rs, l16 + 8, fb, AUX_SC1);
#pragma unroll
          for (int e = 0; e < 3; ++e)
            __builtin_amdgcn_raw_buffer_store_b128(v4u{hp[m][2 * e + 1][0], hp[m][2 * e + 1][1], hp[m][2 * e + 2][0], hp[m][2 * e + 2][1]}, rs, l16, fb + (e + 1) * 1024, AUX_SC1);
        }
      }
      VMWAIT(0);
      setp(L, wave, lane, 15);
    }
    STAMP(9);
    if (wave == 0) {
      waitp(L, 0, 15);
      if (lane == 0) __hip_atomic_store(flags + (3 * 16 + gi) * 16 + gj, tag, RLX_AGENT);
      poll_flags(flags + (3 * 16 + gi) * 16 + (lane & 15), lane < 16, tag);
      setp(L, wave, lane, 16);
    } else waitp1(L, 0, 16);
    STAMP(10);
    {
      constexpr int W = 24;
      v4u a[W];
      const int ab = WS_FF + (gi * 8 + mt3) * 54 * 1024;
#define KS5(s) ((s) < 30 ? ((s) + r5 >= 30 ? (s) + r5 - 30 : (s) + r5) : (s))
#define SL5(s) ((s) < 30 ? KS5(s) : KS5((s) - 30))
#pragma unroll
      for (int s = 0; s < W; ++s) a[s] = __builtin_amdgcn_raw_buffer_load_b128(rs, l16, ab + KS5(s) * 1024, AUX_SC1);
      SCHED();
      f32x4 acc[5];
#pragma unroll
      for (int n = 0; n < 5; ++n) acc[n] = zero;
      LAS char* bb = L + L_W2 + l16;
      waitp(L, 1, 18);
      STAMP(20);
      v4u bf[2][5];
      {
        LAS char* b = bb + SL5(0) * 5120;
#pragma unroll
        for (int n = 0; n < 5; ++n) bf[0][n] = *(LAS v4u*)(b + n * 1024);
      }
#pragma unroll
      for (int s = 0; s < 54; ++s) {
        if (s == 29) waitp(L, 1, 19);
        if (s == 41) waitp(L, 1, 20);
        if (s + 1 < 54) {
          LAS char* b = bb + SL5(s + 1) * 5120;
#pragma unroll
          for (int n = 0; n < 5; ++n) bf[(s + 1) & 1][n] = *(LAS v4u*)(b + n * 1024);
        }
        const v4u a0 = a[s % W];
        if (s + W < 54) a[s % W] = __builtin_amdgcn_raw_buffer_load_b128(rs, l16, ab + KS5(s + W) * 1024, AUX_SC1);
#pragma unroll
        for (int n = 0; n < 5; ++n) acc[n] = mfma(bf[s & 1][n], a0, acc[n]);
#pragma unroll
        for (int n = 0; n < 5; ++n) { SGB(0x8, 1); SGB(0x100, 1); }
        SCHED();
        if (s == 11) setp(L, wave, lane, 17);
        if (s == 23) setp(L, wave, lane, 18);
      }
      GAS char* orow = (GAS char*)P.out + (size_t)(128 * gi + 16 * mt3 + tok) * 1280 + ct * 160 + g4 * 8;
#pragma unroll
      for (int n = 0; n < 5; ++n) {
        v2u o;
        o.x = pk2(bflo(hres[2 * n]) + rbf(acc[n][0]), bfhi(hres[2 * n]) + rbf(acc[n][1]));
        o.y = pk2(bflo(hres[2 * n + 1]) + rbf(acc[n][2]), bfhi(hres[2 * n + 1]) + rbf(acc[n][3]));
        *(GAS v2u*)(orow + n * 32) = o;
      }
    }
  } else {
    {
      const char* src = P.ws + WS_WOT + ct * 5120;
#pragma unroll
      for (int e = 0; e < 3; ++e) { int u = 90 + v + 4 * e; u = u > 99 ? 99 : u; const int ks = u / 5; dma16(src + ks * 40960 + (u - 5 * ks) * 1024, l16, lds0 + L_WO + u * 1024); }
    }
    {
      v4u wa[14];
#pragma unroll
      for (int e = 0; e < 14; ++e) {
        const int u = v + 4 * e, ks = u / 7;
        wa[e] = __builtin_amdgcn_raw_buffer_load_b128(rs, l16, WS_W1T + (ks * 108 + T0 + (u - 7 * ks)) * 1024, AUX_SC1);
      }
      SCHED();
      VMWAIT(14);
      setp(L, wave, lane, 13);
      if (wave == 7) {
        v4u cp[20];
#pragma unroll
        for (int k = 0; k < 20; ++k) cp[k] = __builtin_amdgcn_raw_buffer_load_b128(rs, (lane < 28 ? lane : 0) * 16, WS_CS1 + (k * 1792 + 16 * T0) * 4, AUX_SC1);
        f32x4 c4 = zero;
#pragma unroll
        for (int k = 0; k < 20; ++k) c4 += __builtin_bit_cast(f32x4, cp[k]);
        if (lane < 28) *(LAS f32x4*)(L + L_CS + lane * 16) = c4;
      }
#pragma unroll
      for (int e = 0; e < 14; ++e) *(LAS v4u*)(L + L_W1A + (v + 4 * e) * 1024 + l16) = wa[e];
    }
    STAMP(6);
#define W2LD(u) __builtin_amdgcn_raw_buffer_load_b128(rs, l16, WS_W2T + (((u) / 5) * 40 + 5 * ct + ((u) - 5 * ((u) / 5))) * 1024, AUX_SC1)
    v4u wb[19], wc[19], wd[15];
    waitps(L, 0, 8);
#pragma unroll
    for (int e = 0; e < 19; ++e) wb[e] = W2LD(v + 4 * e);
    SCHED();
    waitp(L, 0, 9);
#pragma unroll
    for (int e = 0; e < 21; ++e) *(LAS v4u*)(L + L_W1B + (v + 4 * e) * 1024 + l16) = pre[e];
    setp(L, wave, lane, 15);
    STAMP(7);
    waitp(L, 0, 10);
#pragma unroll
    for (int e = 0; e < 19; ++e) { int u = 76 + v + 4 * e; u = u > 149 ? 149 : u; wc[e] = W2LD(u); }
    SCHED();
    STAMP(8);
    waitps(L, 0, 13);
#pragma unroll
    for (int e = 0; e < 15; ++e) wd[e] = W2LD(150 + v + 4 * e);
    SCHED();
    waitp(L, 0, 14);
#pragma unroll
    for (int e = 0; e < 19; ++e) *(LAS v4u*)(L + L_W2 + (v + 4 * e) * 1024 + l16) = wb[e];
#pragma unroll
    for (int e = 0; e < 19; ++e) { const int u = 76 + v + 4 * e; if (u < 150) *(LAS v4u*)(L + L_W2 + u * 1024 + l16) = wc[e]; }
    setp(L, wave, lane, 18);
    STAMP(10);
    waitps(L, 0, 15);
#pragma unroll
    for (int e = 0; e < 15; ++e) wb[e] = W2LD(210 + v + 4 * e);
    SCHED();
    waitp(L, 0, 17);
#pragma unroll
    for (int e = 0; e < 15; ++e) {
      const int u = v + 4 * e, ks = u / 5; int sl = ks + r5; sl = sl >= 30 ? sl - 30 : sl;
      *(LAS v4u*)(L + L_W2 + sl * 5120 + (u - 5 * ks) * 1024 + l16) = wd[e];
    }
    setp(L, wave, lane, 19);
    waitp(L, 0, 18);
#pragma unroll
    for (int e = 0; e < 15; ++e) {
      const int u = 60 + v + 4 * e, ks = u / 5; int sl = ks + r5; sl = sl >= 30 ? sl - 30 : sl;
      *(LAS v4u*)(L + L_W2 + sl * 5120 + (u - 5 * ks) * 1024 + l16) = wb[e];
    }
    setp(L, wave, lane, 20);
  }
  STAMP(11);
}
}

extern "C" void kernel_launch(void* const* d_in, const int* in_sizes, int n_in, void* d_out, int out_size, void* d_ws,
                              size_t ws_size, hipStream_t stream) {
  fb::Params P;
  P.x = (const char*)d_in[0]; P.wqkv = (const char*)d_in[1]; P.wo = (const char*)d_in[2];
  P.w1 = (const char*)d_in[4]; P.w2 = (const char*)d_in[5]; P.out = (char*)d_out; P.ws = (char*)d_ws;
  (void)hipFuncSetAttribute((const void*)fb::fused_block, hipFuncAttributeMaxDynamicSharedMemorySize, fb::LDS_BYTES);
  fb::fused_block<<<256, 512, fb::LDS_BYTES, stream>>>(P);
}
```

```cpp
#include <hip/hip_runtime.h>
#include <hip/hip_bf16.h>
#include <stdint.h>

typedef unsigned short u16;
typedef unsigned int u32;
typedef unsigned long long u64;
typedef u32 v4u __attribute__((ext_vector_type(4)));
typedef u32 v2u __attribute__((ext_vector_type(2)));
typedef float f32x4 __attribute__((ext_vector_type(4)));
typedef float f32x2 __attribute__((ext_vector_type(2)));
typedef float f32x16 __attribute__((ext_vector_type(16)));
typedef short s16x4 __attribute__((ext_vector_type(4)));
typedef __bf16 bf16x8 __attribute__((ext_vector_type(8)));
typedef __bf16 bf16x2 __attribute__((ext_vector_type(2)));
#define LAS __attribute__((address_space(3)))
#define GAS __attribute__((address_space(1)))
#define DI __device__ __forceinline__
#define RLX_AGENT __ATOMIC_RELAXED, __HIP_MEMORY_SCOPE_AGENT

#define STAMPS 0
#define CODE_PF 1

namespace fb {
constexpr int WS_EPOCH = 0;
constexpr int WS_FLAGS = 4096;
constexpr int WS_INBOX = 16384;
constexpr int WS_IMG = WS_INBOX + 4 * 256 * 128;
constexpr int WS_OF = WS_IMG + 3 * 128 * 20480;
constexpr int WS_HF = WS_OF + 16 * 8 * 20 * 1024;
constexpr int WS_ST = WS_HF + 16 * 8 * 20 * 1024;
constexpr int WS_FF = WS_ST + 2048 * 64;
constexpr int WS_WOT = WS_FF + 16 * 8 * 54 * 1024;
constexpr int WS_W1T = WS_WOT + 20 * 40 * 1024;
constexpr int WS_W2T = WS_W1T + 20 * 108 * 1024;
constexpr int WS_CS1 = WS_W2T + 54 * 40 * 1024;
constexpr int WS_ST0 = WS_CS1 + 20 * 1792 * 4;
constexpr int WS_END = WS_ST0 + 2048 * 8;
constexpr int WS_STAMP = WS_END;
constexpr int LDS_BYTES = 163840;
constexpr int L_RING = 0;
constexpr int L_XR = 81920;
constexpr int L_UNIT = 147456;
constexpr int L_ST1 = 159744, L_EP = 161792;
constexpr int L_TAB = 162816;
constexpr int L_CS = L_TAB, L_PROG = L_TAB + 512;
constexpr int L_WO = 0;
constexpr int L_K = 92160;
constexpr int L_V = L_K + 24 * 1280, V_TS = 1616;
constexpr int L_W1A = 102400;
constexpr int L_W1B = 0;
constexpr int L_W2 = 0;
constexpr int AUX_SC1 = 16;
constexpr u32 SPIN_CAP = 1u << 17;

DI f32x4 mfma(v4u a, v4u b, f32x4 c) {
  return __builtin_amdgcn_mfma_f32_16x16x32_bf16(__builtin_bit_cast(bf16x8, a), __builtin_bit_cast(bf16x8, b), c, 0, 0, 0);
}
DI f32x16 mfma32(v4u a, v4u b, f32x16 c) {
  return __builtin_amdgcn_mfma_f32_32x32x16_bf16(__builtin_bit_cast(bf16x8, a), __builtin_bit_cast(bf16x8, b), c, 0, 0, 0);
}
DI float max3(float a, float b, float c) { return __builtin_fmaxf(__builtin_fmaxf(a, b), c); }
DI v2u trr(LAS char* p) { return __builtin_bit_cast(v2u, __builtin_amdgcn_ds_read_tr16_b64_v4i16((LAS s16x4*)p)); }
DI v4u trfrag(LAS char* p0, LAS char* p1) { v2u a = trr(p0), b = trr(p1); return v4u{a.x, a.y, b.x, b.y}; }
DI u32 pk2(float a, float b) { f32x2 v = {a, b}; return __builtin_bit_cast(u32, __builtin_convertvector(v, bf16x2)); }
DI float bflo(u32 u) { return __uint_as_float(u << 16); }
DI float bfhi(u32 u) { return __uint_as_float(u & 0xFFFF0000u); }
DI float rbf(float f) { return bflo(pk2(f, 0.f)); }
DI float dot2(u32 a, u32 b, float c) {
  return __builtin_amdgcn_fdot2_f32_bf16(__builtin_bit_cast(bf16x2, a), __builtin_bit_cast(bf16x2, b), c, false);
}
DI float sum4(float v) {
  auto r = __builtin_amdgcn_permlane16_swap(__float_as_uint(v), __float_as_uint(v), false, false);
  v = __uint_as_float(r[0]) + __uint_as_float(r[1]);
  auto s = __builtin_amdgcn_permlane32_swap(__float_as_uint(v), __float_as_uint(v), false, false);
  return __uint_as_float(s[0]) + __uint_as_float(s[1]);
}
DI float max4(float v) {
  auto r = __builtin_amdgcn_permlane16_swap(__float_as_uint(v), __float_as_uint(v), false, false);
  v = fmaxf(__uint_as_float(r[0]), __uint_as_float(r[1]));
  auto s = __builtin_amdgcn_permlane32_swap(__float_as_uint(v), __float_as_uint(v), false, false);
  return fmaxf(__uint_as_float(s[0]), __uint_as_float(s[1]));
}
DI void dma16(const void* sbase, u32 voff, u32 lds_dst) {
  u32 keep;
  asm volatile("s_mov_b32 %0, m0\n\ts_mov_b32 m0, %3\n\ts_nop 0\n\tglobal_load_lds_dwordx4 %1, %2\n\ts_mov_b32 m0, %0"
               : "=&s"(keep) : "v"(voff), "s"(sbase), "s"(__builtin_amdgcn_readfirstlane(lds_dst)) : "memory");
}
typedef int v4i __attribute__((ext_vector_type(4)));
DI v4i mkrsrc(const void* p, int bytes) { return v4i{(int)(u32)(uintptr_t)p, (int)((uintptr_t)p >> 32), bytes, 0x00020000}; }
DI void dmab(v4i rsrc, u32 voff, u32 soff, u32 lds_dst) {
  asm volatile("s_mov_b32 m0, %3\n\ts_nop 0\n\tbuffer_load_dwordx4 %0, %1, %2 offen lds"
               :: "v"(voff), "s"(rsrc), "s"(__builtin_amdgcn_readfirstlane(soff)), "s"(__builtin_amdgcn_readfirstlane(lds_dst)) : "memory", "m0");
}
DI void dmas(v4i rsrc, u32 voff, u32 soff, u32 lds_dst) {
  asm volatile("s_mov_b32 m0, %3\n\ts_nop 0\n\tbuffer_load_dwordx4 %0, %1, %2 offen sc1 lds"
               :: "v"(voff), "s"(rsrc), "s"(__builtin_amdgcn_readfirstlane(soff)), "s"(__builtin_amdgcn_readfirstlane(lds_dst)) : "memory", "m0");
}
DI void dmas_nc(v4i rsrc, u32 voff, u32 soff, u32 lds_dst) {
  asm volatile("s_mov_b32 m0, %3\n\ts_nop 0\n\tbuffer_load_dwordx4 %0, %1, %2 offen sc1 lds"
               :: "v"(voff), "s"(rsrc), "s"(__builtin_amdgcn_readfirstlane(soff)), "s"(__builtin_amdgcn_readfirstlane(lds_dst)) : "m0");
}
#define VMWAIT(n) asm volatile("s_waitcnt vmcnt(" #n ")" ::: "memory")
#define BAR() do { asm volatile("s_waitcnt lgkmcnt(0)" ::: "memory"); __builtin_amdgcn_s_barrier(); asm volatile("" ::: "memory"); } while (0)
#define SCHED() __builtin_amdgcn_sched_barrier(0)
#define SGB(mask, n) __builtin_amdgcn_sched_group_barrier(mask, n, 0)
DI LAS char* opaque(LAS char* p) { asm volatile("" : "+v"(p)); return p; }

DI u64 mix64(u64 z) {
  z += 0x9E3779B97F4A7C15ull; z = (z ^ (z >> 30)) * 0xBF58476D1CE4E5B9ull; z = (z ^ (z >> 27)) * 0x94D049BB133111EBull;
  return z ^ (z >> 31);
}
DI void poll_flags(GAS u64* p, bool active, u64 tag) {
  for (u32 spins = 0; spins < SPIN_CAP; ++spins) {
    u64 v = tag;
    if (active) v = __hip_atomic_load(p, RLX_AGENT);
    if (__all(v == tag)) break;
    __builtin_amdgcn_s_sleep(1);
  }
  __builtin_amdgcn_fence(__ATOMIC_ACQUIRE, "wavefront");
}

DI void setp(LAS char* L, int wave, int lane, u32 step) {
  asm volatile("s_waitcnt lgkmcnt(0)" ::: "memory");
  if (lane == 0) *(volatile LAS u32*)(L + L_PROG + wave * 4) = step;
  asm volatile("" ::: "memory");
}
DI void waitp(LAS char* L, int grp, u32 step) {
  for (u32 spins = 0; spins < (1u << 24); ++spins) {
    const v4u x = *(volatile LAS v4u*)(L + L_PROG + grp * 16);
    const u32 m = min(min(x.x, x.y), min(x.z, x.w));
    if (__builtin_amdgcn_readfirstlane(m) >= step) break;
  }
  asm volatile("" ::: "memory");
}
DI void waitps(LAS char* L, int grp, u32 step) {
  for (u32 spins = 0; spins < (1u << 22); ++spins) {
    const v4u x = *(volatile LAS v4u*)(L + L_PROG + grp * 16);
    const u32 m = min(min(x.x, x.y), min(x.z, x.w));
    if (__builtin_amdgcn_readfirstlane(m) >= step) break;
    __builtin_amdgcn_s_sleep(2);
  }
  asm volatile("" ::: "memory");
}
DI void waitp2(LAS char* L, int w, u32 step) {
  for (u32 spins = 0; spins < (1u << 24); ++spins) {
    const v2u x = *(volatile LAS v2u*)(L + L_PROG + w * 4);
    if (__builtin_amdgcn_readfirstlane(min(x.x, x.y)) >= step) break;
  }
  asm volatile("" ::: "memory");
}
DI void waitp1(LAS char* L, int w, u32 step) {
  for (u32 spins = 0; spins < (1u << 24); ++spins) {
    const u32 x = *(volatile LAS u32*)(L + L_PROG + w * 4);
    if (__builtin_amdgcn_readfirstlane(x) >= step) break;
  }
  asm volatile("" ::: "memory");
}

struct Params {
  const char* x; const char* wqkv; const char* wo; const char* w1; const char* w2; char* out; char* ws;
};

#define STAMP(k) do {} while (0)

#define LST(k)
#define RAMP(i, W0, W) ((W0) + 2 * (i) < (i) + (W) - 1 ? (W0) + 2 * (i) : (i) + (W) - 1)
__global__ void __launch_bounds__(512, 2) fused_block(Params P) {
  extern __shared__ __attribute__((aligned(16))) char smem[];
  LAS char* L = (LAS char*)smem;
  const u32 lds0 = (u32)(uintptr_t)L;
  const int tid = threadIdx.x, lane = tid & 63;
  const int wave = __builtin_amdgcn_readfirstlane(tid >> 6);
  const int bid = blockIdx.x;
  const int gi = (bid & 7) * 2 + (bid >> 7);
  const int gj = (bid >> 3) & 15;
  const int tok = lane & 15, g4 = lane >> 4, q4 = (lane & 15) >> 2, p4 = lane & 3;
  const bool isM = wave < 4;
  const int v = wave & 3;
  const __amdgpu_buffer_rsrc_t rs = __builtin_amdgcn_make_buffer_rsrc((void*)P.ws, 0, WS_END, 0x00020000);
  GAS u64* flags = (GAS u64*)(P.ws + WS_FLAGS);
  GAS u64* inbox = (GAS u64*)(P.ws + WS_INBOX);
  const int me = gi * 16 + gj;
#define P1MAP 1
  const int ti = ((bid & 7) >> 1) * 4 + ((bid >> 3) & 3), tj = (bid & 1) * 8 + (bid >> 5);
#define ROTM 2
  const int rot = ROTM == 0 ? 0 : (ROTM == 1 ? ((ti + tj) & 1) * 5 : (ti + tj) % 10);
  const int rh = gj >> 3, ct = gj & 7;
  const int T0 = gj < 12 ? 7 * gj : 84 + 6 * (gj - 12);
  const int nT = gj < 12 ? 7 : 6;
  const int r5 = (ct * 30) >> 3;
  const int r3 = 2 * ct, r4 = (gj * 5) >> 2;
#define KS3(p) ((p) < 16 ? (((p) + r3) & 15) : (p))
#define KS4(s) ((s) + r4 >= 20 ? (s) + r4 - 20 : (s) + r4)
#define KS5(s) ((s) < 30 ? ((s) + r5 >= 30 ? (s) + r5 - 30 : (s) + r5) : (s))
  const v4u ones = {0x3F803F80u, 0x3F803F80u, 0x3F803F80u, 0x3F803F80u};
  const f32x4 zero = {0.f, 0.f, 0.f, 0.f};
  const int l16 = lane * 16;
  STAMP(0);
  {
    u64 pc0; u32 len;
    asm volatile("s_getpc_b64 %0\n\ts_mov_b32 %1, fb_code_end-." : "=s"(pc0), "=s"(len));
    if (wave == CODE_PF && len <= 131072u) {
      const u32 per = ((len >> 5) + 1023u) & ~1023u;
      const v4i rc = mkrsrc((const void*)(pc0 & ~63ull), (int)len);
      for (u32 o = (bid >> 3) * per; o < ((bid >> 3) + 1) * per; o += 1024) dmab(rc, o + l16, 0, lds0 + L_ST1);
    }
  }
  if (tid < 8) *(volatile LAS u32*)(L + L_PROG + tid * 4) = 0u;
  const v4i rsi = mkrsrc(P.ws, WS_END);
  if (wave == 0) dmas(rsi, l16, 0, lds0 + L_EP);
  u64 tag;
#define GET_TAG() { const v2u ep = *(volatile LAS v2u*)(L + L_EP); \
    tag = mix64((u64)__builtin_amdgcn_readfirstlane(ep.x) | ((u64)__builtin_amdgcn_readfirstlane(ep.y) << 32)); }
#define MAKE_TAG() { VMWAIT(0); GET_TAG() }
  v4u xv[10];
  if (wave == 0) {
    const GAS char* xr = (const GAS char*)P.x + (size_t)(128 * gi + 8 * gj + (lane >> 3)) * 1280 + (lane & 7) * 16;
#pragma unroll
    for (int e = 0; e < 10; ++e) xv[e] = *(const GAS v4u*)(xr + e * 128);
    SCHED();
  }
#define ROW_STATS() if (wave == 0) { \
    float s1 = 0.f, s2 = 0.f; \
    _Pragma("unroll") for (int e = 0; e < 10; ++e) _Pragma("unroll") for (int k = 0; k < 4; ++k) { s1 = dot2(xv[e][k], 0x3F803F80u, s1); s2 = dot2(xv[e][k], xv[e][k], s2); } \
    _Pragma("unroll") for (int o = 1; o < 8; o <<= 1) { s1 += __shfl_xor(s1, o); s2 += __shfl_xor(s2, o); } \
    const float mean = s1 * (1.f / 640.f); \
    const float var = fmaxf(s2 * (1.f / 640.f) - mean * mean, 0.f); \
    const float rstd = 1.f / rbf(sqrtf(rbf(rbf(var) + 1e-5f))); \
    if ((lane & 7) == 0) \
      __builtin_amdgcn_raw_buffer_store_b64(v2u{__float_as_uint(rbf(mean)), __float_as_uint(rstd)}, rs, (lane >> 3) * 8, WS_ST0 + (128 * gi + 8 * gj) * 8, AUX_SC1); }
#define ROW_STATS_FLAG() if (wave == 0) { MAKE_TAG() if (lane == 0) __hip_atomic_store(flags + (5 * 16 + gi) * 16 + gj, tag, RLX_AGENT); }
  bool f1act; GAS u64* f1dst;
  {
    const int sec = tj / 5, jj = tj - 5 * sec, a = lane >> 2, g = (32 * jj) / 10 + (lane & 3), ci = ti + a;
    f1act = lane < 12 && tj != 15 && ci < 16 && (sec != 0 || a == 0);
      const int slot = 2 * (sec == 0 ? 0 : (sec == 1 ? 3 : 6) - a) + (((10 * g) >> 5) == jj ? 0 : 1);
    f1dst = f1act ? inbox + (ci * 16 + g) * 16 + slot : flags + ti * 16 + tj;
    asm volatile("" : "+v"(f1dst));
  }
  if (tj == 15) {
    const int R = ti * 8 + wave;
    LAS char* stg = L + wave * 4096;
    const int srow = lane >> 3, sc = (lane & 7) * 16;
    LAS char* tb = stg + (4 * g4 + q4) * 128 + p4 * 8;
    v4u d[3][4];
    const __amdgpu_buffer_rsrc_t rwo = __builtin_amdgcn_make_buffer_rsrc((void*)P.wo, 0, 640 * 1280, 0x00020000);
    const __amdgpu_buffer_rsrc_t rw1 = __builtin_amdgcn_make_buffer_rsrc((void*)P.w1, 0, 640 * 3456, 0x00020000);
#pragma unroll
    for (int u = 0; u < 3; ++u) {
      const int U = R + 128 * u;
      if (u < 2 && U < 200) {
        const int ks = U / 10, lg = U - ks * 10;
#pragma unroll
        for (int e = 0; e < 4; ++e) {
          const int rho = 8 * e + srow, t = rho >> 4, gg = (rho >> 2) & 3, qq = rho & 3;
          const int row = ks < 16 ? (2 * ks + (gg >> 1)) * 20 + 8 * t + 4 * (gg & 1) + qq : (2 * (4 * (ks - 16) + gg) + t) * 20 + 16 + qq;
          d[u][e] = __builtin_amdgcn_raw_buffer_load_b128(rwo, row * 1280 + sc, lg * 128, 0);
        }
      } else {
        const int U1 = U - 200, ks = U1 / 27, cg = U1 - ks * 27;
#pragma unroll
        for (int e = 0; e < 4; ++e) d[u][e] = __builtin_amdgcn_raw_buffer_load_b128(rw1, (8 * e + srow) * 3456 + sc, ks * (32 * 3456) + cg * 128, 0);
      }
    }
    SCHED();
    BAR();
    ROW_STATS()
    ROW_STATS_FLAG()
#pragma unroll
    for (int u = 0; u < 3; ++u) {
      const int U = R + 128 * u;
#pragma unroll
      for (int e = 0; e < 4; ++e) *(LAS v4u*)(stg + e * 1024 + l16) = d[u][e];
      if (u < 2 && U < 200) {
        const int ks = U / 10, lg = U - ks * 10;
#pragma unroll
        for (int n = 0; n < 4; ++n)
          __builtin_amdgcn_raw_buffer_store_b128(trfrag(tb + n * 32, tb + n * 32 + 2048), rs, l16, WS_WOT + (ks * 40 + 4 * lg + n) * 1024, AUX_SC1);
      } else {
        const int U1 = U - 200, ks = U1 / 27, cg = U1 - ks * 27;
        f32x4 c4[4];
#pragma unroll
        for (int n = 0; n < 4; ++n) {
          const v4u f = trfrag(tb + n * 32, tb + n * 32 + 2048);
          __builtin_amdgcn_raw_buffer_store_b128(f, rs, l16, WS_W1T + (ks * 108 + 4 * cg + n) * 1024, AUX_SC1);
          c4[n] = mfma(f, ones, zero);
        }
        const f32x4 cv = tok == 0 ? c4[0] : (tok == 1 ? c4[1] : (tok == 2 ? c4[2] : c4[3]));
        if (tok < 4) __builtin_amdgcn_raw_buffer_store_b128(__builtin_bit_cast(v4u, cv), rs, tok * 64 + g4 * 16, WS_CS1 + (ks * 1792 + cg * 64) * 4, AUX_SC1);
      }
      if (u == 1) {
        VMWAIT(0);
        BAR();
        GET_TAG()
        if (tid == 0) __hip_atomic_store(flags + (4 * 16 + 0) * 16 + ti, tag, RLX_AGENT);
      }
    }
    VMWAIT(0);
    STAMP(1);
    BAR();
    if (tid == 0) __hip_atomic_store(flags + (4 * 16 + 1) * 16 + ti, tag, RLX_AGENT);
    if (isM) setp(L, wave, lane, 13);
  } else if (isM) {
    BAR();
    ROW_STATS()
    const int mh = wave >> 1, nh = wave & 1;
    f32x4 acc1[4][4];
#pragma unroll
    for (int m = 0; m < 4; ++m)
#pragma unroll
      for (int n = 0; n < 4; ++n) acc1[m][n] = zero;
    f32x4 cs[2] = {zero, zero};
    LAS char* bn[4];
#pragma unroll
    for (int n = 0; n < 4; ++n) bn[n] = L + L_RING + (8 * g4 + q4) * 256 + (((4 * nh + (n ^ (2 * mh))) ^ (q4 | ((g4 & 1) << 2))) * 32) + p4 * 8;
    LAS char* an[2];
#pragma unroll
    for (int k = 0; k < 2; ++k) an[k] = L + L_XR + (64 * mh + tok) * 128 + (((4 * k + g4) ^ (tok & 7)) * 16);
    v4u b[2][4], a[2][4], pg;
    u64 sfl; v2u stv[4];
    waitp(L, 1, 1);
#pragma unroll
    for (int n = 0; n < 4; ++n) { b[0][n] = trfrag(bn[n], bn[n] + 1024); a[0][n] = *(LAS v4u*)(an[0] + n * 2048); }
#pragma unroll
    for (int s = 0; s < 20; ++s) {
      if (s + 1 < 20) {
        const int c1 = (s + 1) >> 1, k1 = (s + 1) & 1, sl = c1 % 5;
        if (k1 == 1) pg = *(volatile LAS v4u*)(L + L_PROG + 16);
        if (k1 == 0) {
          if (__builtin_amdgcn_readfirstlane(min(min(pg.x, pg.y), min(pg.z, pg.w))) < (u32)(c1 + 1)) waitp(L, 1, c1 + 1);
          asm volatile("" ::: "memory");
          if (sl == 4 || sl == 0) {
#pragma unroll
            for (int n = 0; n < 4; ++n) bn[n] = opaque(bn[n] + (sl == 4 ? 65536 : -65536));
          }
        }
        const int so = (sl & 3) * 16384 + k1 * 8192;
#pragma unroll
        for (int n = 0; n < 4; ++n) {
          b[(s + 1) & 1][n] = trfrag(bn[n] + so, bn[n] + so + 1024);
          a[(s + 1) & 1][n] = *(LAS v4u*)(an[k1] + (c1 & 3) * 16384 + n * 2048);
        }
      }
#pragma unroll
      for (int n = 0; n < 4; ++n)
#pragma unroll
        for (int m = 0; m < 4; ++m) acc1[m][n] = mfma(b[s & 1][n], a[s & 1][m], acc1[m][n]);
      cs[0] = mfma(b[s & 1][0], ones, cs[0]); cs[1] = mfma(b[s & 1][1], ones, cs[1]);
#pragma unroll
      for (int n = 0; n < 13; ++n) { SGB(0x8, 1); SGB(0x100, 1); }
      SCHED();
      if ((s & 1) == 1 && s < 19 && lane == 0) *(volatile LAS u32*)(L + L_PROG + wave * 4) = (u32)(s >> 1) + 1u;
      if (s == 3) ROW_STATS_FLAG()
      if (s == 11) sfl = __hip_atomic_load(flags + (5 * 16 + ti) * 16 + tok, RLX_AGENT);
      if (s == 15) {
        GET_TAG()
        if (!__all(sfl == tag)) poll_flags(flags + (5 * 16 + ti) * 16 + tok, true, tag);
        __builtin_amdgcn_fence(__ATOMIC_ACQUIRE, "wavefront");
#pragma unroll
        for (int m = 0; m < 4; ++m) stv[m] = __builtin_amdgcn_raw_buffer_load_b64(rs, (16 * m + tok) * 8, WS_ST0 + (128 * ti + 64 * mh) * 8, AUX_SC1);
      }
    }
    if (tok == 0) {
      *(LAS f32x4*)(L + L_CS + (64 * nh + 32 * mh + 4 * g4) * 4) = cs[0];
      *(LAS f32x4*)(L + L_CS + (64 * nh + 32 * mh + 16 + 4 * g4) * 4) = cs[1];
    }
    setp(L, wave, lane, 10);
    STAMP(1);
    const int sec = tj / 5, jj = tj - 5 * sec;
    const float qs = sec == 0 ? 0.22360679774997896f * 1.4426950408889634f : 1.f;
    float mb[4], rstd[4];
#pragma unroll
    for (int m = 0; m < 4; ++m) {
      mb[m] = __uint_as_float(stv[m].x); rstd[m] = __uint_as_float(stv[m].y) * qs;
    }
    waitp(L, 0, 10);
    const int so = WS_IMG + ((sec * 128 + 8 * ti + 4 * mh) * 80 + 16 * jj) * 256;
    const int vo = (2 * (g4 & 1) + (g4 >> 1)) * 256 + tok * 16;
#pragma unroll
    for (int e = 0; e < 2; ++e) {
      const int tp = 2 * nh + (e ^ mh);
      const f32x4 c0 = *(LAS f32x4*)(L + L_CS + (32 * tp + 4 * g4) * 4), c1 = *(LAS f32x4*)(L + L_CS + (32 * tp + 16 + 4 * g4) * 4);
#pragma unroll
      for (int m = 0; m < 4; ++m) {
        const f32x4 x = acc1[m][2 * e], y = acc1[m][2 * e + 1];
        const u32 x0 = pk2((x[0] - mb[m] * c0[0]) * rstd[m], (x[1] - mb[m] * c0[1]) * rstd[m]);
        const u32 x1 = pk2((x[2] - mb[m] * c0[2]) * rstd[m], (x[3] - mb[m] * c0[3]) * rstd[m]);
        const u32 y0 = pk2((y[0] - mb[m] * c1[0]) * rstd[m], (y[1] - mb[m] * c1[1]) * rstd[m]);
        const u32 y1 = pk2((y[2] - mb[m] * c1[2]) * rstd[m], (y[3] - mb[m] * c1[3]) * rstd[m]);
        auto r0 = __builtin_amdgcn_permlane16_swap(x0, y0, false, false);
        auto r1 = __builtin_amdgcn_permlane16_swap(x1, y1, false, false);
        __builtin_amdgcn_raw_buffer_store_b128(v4u{r0[0], r1[0], r0[1], r1[1]}, rs, vo, so + m * 20480 + tp * 1024, AUX_SC1);
      }
    }
    STAMP(14);
    VMWAIT(0);
    setp(L, wave, lane, 13);
  } else {
    const bool isX = wave >= 6;
    const int lv = wave & 1, D = isX ? 4 : 5;
    const v4i rl = isX ? mkrsrc(P.x, 2048 * 1280) : mkrsrc(P.wqkv, 640 * 3840);
    u32 vo[2];
#pragma unroll
    for (int k = 0; k < 2; ++k)
      vo[k] = isX ? (lane >> 3) * 1280 + (((lane & 7) ^ (lane >> 3)) * 16) : g4 * 3840 + (((tok >> 1) ^ (g4 | (k << 2))) * 32) + (tok & 1) * 16;
    const u32 sb = isX ? (128 * ti + 64 * lv) * 1280 : tj * 256 + (32 * lv) * 3840;
    const u32 cstr = isX ? 128 : 64 * 3840, pstr = isX ? 8 * 1280 : 4 * 3840;
    const u32 db = lds0 + (isX ? L_XR : L_RING) + lv * 8192;
    int slot = 0, kc = rot;
#define L_ISSUE() { _Pragma("unroll") for (int e = 0; e < 8; ++e) dmab(rl, vo[(e >> 1) & 1], sb + kc * cstr + e * pstr, db + slot * 16384 + e * 1024); \
      slot = slot + 1 == D ? 0 : slot + 1; kc = kc == 9 ? 0 : kc + 1; }
    L_ISSUE()
    BAR();
#pragma unroll 1
    for (int c = 1; c < 10; ++c) {
      if (c >= D) waitp(L, 0, c - D + 1);
      L_ISSUE()
      if (c < 9) { VMWAIT(8); if (lane == 0) *(volatile LAS u32*)(L + L_PROG + wave * 4) = (u32)c; }
    }
    const int U = (ti * 15 + tj) * 4 + v;
    const bool isW2 = U < 540, hasU = U < 896;
    const int uks = isW2 ? U / 10 : (hasU ? (U - 356) / 27 : 0), ulg = isW2 ? U - uks * 10 : (hasU ? (U - 356) - uks * 27 : 0);
    {
      const v4i ru = isW2 ? mkrsrc(P.w2, 1728 * 1280) : mkrsrc(P.w1, 640 * 3456);
      const int rstr = isW2 ? 1280 : 3456;
      const u32 sd = lds0 + (v < 3 ? L_UNIT + v * 4096 : L_XR + 2 * 16384);
      if (v == 3) waitp(L, 0, 7);
#pragma unroll
      for (int e = 0; e < 4; ++e) dmab(ru, (8 * e + (lane >> 3)) * rstr + (lane & 7) * 16, uks * 32 * rstr + ulg * 128, sd + e * 1024);
      VMWAIT(12); if (lane == 0) *(volatile LAS u32*)(L + L_PROG + wave * 4) = 9u;
      VMWAIT(4); if (lane == 0) *(volatile LAS u32*)(L + L_PROG + wave * 4) = 10u;
      VMWAIT(0);
      if (hasU) {
        LAS char* tb = L + (sd - lds0) + (4 * g4 + q4) * 128 + p4 * 8;
        const int dst = isW2 ? WS_W2T + (uks * 40 + 4 * ulg) * 1024 : WS_W1T + (uks * 108 + 4 * ulg) * 1024;
        f32x4 c4[4];
#pragma unroll
        for (int n = 0; n < 4; ++n) {
          const v4u f = trfrag(tb + n * 32, tb + n * 32 + 2048);
          __builtin_amdgcn_raw_buffer_store_b128(f, rs, l16, dst + n * 1024, AUX_SC1);
          c4[n] = mfma(f, ones, zero);
        }
        const f32x4 cv = tok == 0 ? c4[0] : (tok == 1 ? c4[1] : (tok == 2 ? c4[2] : c4[3]));
        if (tok < 4 && !isW2) __builtin_amdgcn_raw_buffer_store_b128(__builtin_bit_cast(v4u, cv), rs, tok * 64 + g4 * 16, WS_CS1 + (uks * 1792 + ulg * 64) * 4, AUX_SC1);
      }
    }
    VMWAIT(0);
    setp(L, wave, lane, 12);
    STAMP(1);
  }
  STAMP(2);
  const int hh = wave & 1, qg = wave >> 1;
  u64 fl[5];
  if (wave == 0) {
    {
      waitp(L, 0, 13);
      if (tj != 15) waitp(L, 1, 12);
      if (f1act || lane == 12) __hip_atomic_store(f1dst, tag, RLX_AGENT);
    }
    STAMP(15);
  }
  int go[8], lo[8];
  {
    const int isV = wave >> 2, t0 = 6 * (wave & 3);
    const int tmin = 16 - 8 * gi - t0;
#pragma unroll
    for (int e = 0; e < 8; ++e) {
      const int x = e * 64 + lane, t4 = x / 80, rem = x - t4 * 80;
      const bool ok = x < 480 && t4 >= tmin;
      lo[e] = ok ? (isV ? L_V + (t0 + t4) * V_TS + (rem >> 4) * 320 + tok * 16 : L_K + t0 * 1280 + x * 16) : L_CS + (lane & 15) * 16;
      go[e] = ok ? t4 * 20480 + rem * 16 : 0x7FFFFFF0;
    }
  }
  const int qo = ((8 * gi + 2 * qg + ((lane & 31) >> 4)) * 80 + 5 * gj) * 256 + tok * 16;
  const int r32 = lane & 31, h = lane >> 5, dg = (lane >> 4) & 1;
  const float NEG = -__builtin_inff();
  LAS char* k1p = L + L_K + (2 * qg + (r32 >> 4)) * 1280 + (r32 & 15) * 16;
  LAS char* k0p = k1p + (3 * hh + h) * 256;
  k1p += 512;
  LAS char* vp[2];
#pragma unroll
  for (int t = 0; t < 2; ++t) {
    const int key = 8 * t + 4 * h + q4, gam = 5 * hh + (dg ? 4 : p4);
    const int off = (dg && p4) ? 1600 : (gam >> 1) * 320 + key * 16 + (gam & 1) * 8;
    vp[t] = L + L_V + (2 * qg) * V_TS + off;
  }
  k0p = opaque(k0p); k1p = opaque(k1p); vp[0] = opaque(vp[0]); vp[1] = opaque(vp[1]);
  if (wave == 0) {
    const int slot = lane >> 1;
    const int row = slot == 0 ? gi : gi - 2 + (slot < 4 ? slot - 1 : slot - 4);
    const bool act = lane < 14 && row >= 0 && ((lane & 1) == 0 || ((10 * gj) >> 5) != ((10 * gj + 9) >> 5));
    poll_flags(inbox + me * 16 + (lane & 15), act, tag);
    waitp1(L, 4, 13);
    setp(L, wave, lane, 14);
  } else if (wave == 4) {
    GET_TAG()
    poll_flags(flags + (4 * 16 + 0) * 16 + tok, lane < 16, tag);
    setp(L, wave, lane, 13);
    waitp1(L, 0, 14);
  } else waitp1(L, 0, 14);
  STAMP(3);
  v4u q0, q1;
  {
    const int gb = WS_IMG + (((1 + (wave >> 2)) * 128 + 8 * gi - 16 + 6 * (wave & 3)) * 80 + 5 * gj) * 256;
    v4u hv[8];
#pragma unroll
    for (int e = 0; e < 8; ++e) hv[e] = __builtin_amdgcn_raw_buffer_load_b128(rs, go[e], gb, AUX_SC1);
    q0 = __builtin_amdgcn_raw_buffer_load_b128(rs, qo + (3 * hh + (lane >> 5)) * 256, WS_IMG, AUX_SC1);
    q1 = __builtin_amdgcn_raw_buffer_load_b128(rs, qo + 512, WS_IMG, AUX_SC1);
    if (wave == 7) {
#pragma unroll
      for (int r = 0; r < 4; ++r) fl[r] = __hip_atomic_load(flags + r * 64 + lane, RLX_AGENT);
      fl[4] = __hip_atomic_load(flags + (4 * 16 + 1) * 16 + (lane & 15), RLX_AGENT);
    }
    SCHED();
    STAMP(16);
    if (wave == 2 && lane < 24) *(LAS v4u*)(L + L_V + lane * V_TS + 1600) = ones;
#pragma unroll
    for (int e = 0; e < 8; ++e) *(LAS v4u*)(L + lo[e]) = hv[e];
    GET_TAG()
    if (isM) {
      float km = 0.f;
#pragma unroll
      for (int e = 0; e < 8; ++e) km = fmaxf(km, dot2(hv[e].x, hv[e].x, dot2(hv[e].y, hv[e].y, dot2(hv[e].z, hv[e].z, dot2(hv[e].w, hv[e].w, 0.f)))));
      km = km == km ? km : __builtin_inff();
      km = fmaxf(km, __int_as_float(__builtin_amdgcn_update_dpp(0, __float_as_int(km), 0x128, 0xf, 0xf, false)));
      km = fmaxf(km, __int_as_float(__builtin_amdgcn_update_dpp(0, __float_as_int(km), 0x124, 0xf, 0xf, false)));
      km = fmaxf(km, __int_as_float(__builtin_amdgcn_update_dpp(0, __float_as_int(km), 0x122, 0xf, 0xf, false)));
      km = fmaxf(km, __int_as_float(__builtin_amdgcn_update_dpp(0, __float_as_int(km), 0x121, 0xf, 0xf, false)));
      km = max4(km);
      if (lane == 0) *(LAS float*)(L + L_PROG + 32 + wave * 4) = km;
    }
    if (wave == 7) {
      const bool ok = __all(fl[0] == tag && fl[1] == tag && fl[2] == tag && fl[3] == tag && fl[4] == tag);
      if (lane == 0) *(LAS u32*)(L + L_PROG + 48) = ok ? 1u : 0u;
    }
  }
  if (lane >= 32) q1 = v4u{0u, 0u, 0u, 0u};
  else if (hh == 0) { q1.z = 0u; q1.w = 0u; }
  else { q1.x = 0u; q1.y = 0u; }
  BAR();
  STAMP(4);
  {
    f32x16 o, z16;
#pragma unroll
    for (int i = 0; i < 16; ++i) { o[i] = 0.f; z16[i] = 0.f; }
    const int dq = r32 - 4 * h;
#define UP(i) (((i) & 3) + 8 * ((i) >> 2) > dq)
#define QK(b, c) mfma32(*(LAS v4u*)(k1p + (b) * 2560), q1, mfma32(*(LAS v4u*)(k0p + (b) * 2560), q0, c))
#define MASK_EDGE(s) _Pragma("unroll") for (int i = 0; i < 16; ++i) s[i] = UP(i) ? s[i] : NEG
#define MASK_DIAG(s) _Pragma("unroll") for (int i = 0; i < 16; ++i) s[i] = UP(i) ? NEG : s[i]
#define MAXS(s) _Pragma("unroll") for (int i = 0; i < 16; i += 4) { m = max3(m, s[i], s[i + 1]); m2 = max3(m2, s[i + 2], s[i + 3]); }
#define EX(x) __builtin_amdgcn_exp2f(x)
#define PV(b, s) { const v4u pa = {pk2(EX(s[0]), EX(s[1])), pk2(EX(s[2]), EX(s[3])), pk2(EX(s[4]), EX(s[5])), pk2(EX(s[6]), EX(s[7]))}; \
      const v4u pb = {pk2(EX(s[8]), EX(s[9])), pk2(EX(s[10]), EX(s[11])), pk2(EX(s[12]), EX(s[13])), pk2(EX(s[14]), EX(s[15]))}; \
      o = mfma32(trfrag(vp[0] + (b) * (2 * V_TS), vp[1] + (b) * (2 * V_TS)), pa, o); o = mfma32(trfrag(vp[0] + (b) * (2 * V_TS) + V_TS, vp[1] + (b) * (2 * V_TS) + V_TS), pb, o); }
    if (!isM) __builtin_amdgcn_s_setprio(1);
    const bool flok = __builtin_amdgcn_readfirstlane(*(LAS u32*)(L + L_PROG + 48)) != 0u;
    if (!flok) {
      poll_flags(flags + (4 * 16 + 1) * 16 + (lane & 15), true, tag);
#pragma unroll 1
      for (int r = 0; r < 4; ++r) poll_flags(flags + r * 64 + lane, true, tag);
    }
    if (bid == 0 && tid == 0) __hip_atomic_store((GAS u64*)(P.ws + WS_EPOCH), tag, RLX_AGENT);
    const int wrot = 22 * (2 * (gi & 1) + rh);
#define WO_PIECE(e) { int u = wave + 8 * (e); u = u > 89 ? 89 : u; u += wrot; u = u >= 90 ? u - 90 : u; const int ks = u / 5; \
      dmas_nc(rsi, l16, WS_WOT + ct * 5120 + KS3(ks) * 40960 + (u - 5 * ks) * 1024, lds0 + L_WO + u * 1024); }
    const int bmin = 8 - 4 * gi - qg;
    float m;
    {
      const f32x4 km4 = *(LAS f32x4*)(L + L_PROG + 32);
      float qn = dot2(q0.x, q0.x, dot2(q0.y, q0.y, dot2(q0.z, q0.z, dot2(q0.w, q0.w, dot2(q1.x, q1.x, dot2(q1.y, q1.y, dot2(q1.z, q1.z, dot2(q1.w, q1.w, 0.f))))))));
      auto r = __builtin_amdgcn_permlane32_swap(__float_as_uint(qn), __float_as_uint(qn), false, false);
      qn = __uint_as_float(r[0]) + __uint_as_float(r[1]);
      m = 1.01f * sqrtf(qn * 3.f * max3(fmaxf(km4[0], km4[1]), km4[2], km4[3])) + 0.01f;
    }
    if (!__all(m <= 40.f)) {
      float m2 = NEG;
      m = NEG;
      { f32x16 s = QK(8, z16); MASK_DIAG(s); MAXS(s) }
      if (bmin <= 0) { f32x16 s = QK(0, z16); MASK_EDGE(s); MAXS(s) }
#pragma unroll 1
      for (int b = bmin > 1 ? bmin : 1; b < 8; ++b) { f32x16 s = QK(b, z16); MAXS(s) }
      m = max3(m, m2, m2);
      auto r = __builtin_amdgcn_permlane32_swap(__float_as_uint(m), __float_as_uint(m), false, false);
      m = max3(m, __uint_as_float(r[0]), __uint_as_float(r[1]));
    }
    f32x16 nm;
#pragma unroll
    for (int i = 0; i < 16; ++i) nm[i] = -m;
    if (bmin <= 0) {
      f32x16 s = QK(0, nm);
#pragma unroll
      for (int b = 0; b < 9; ++b) {
        f32x16 sn = s;
        if (b < 8) sn = QK(b + 1, nm);
        if (b == 0) MASK_EDGE(s);
        if (b == 8) MASK_DIAG(s);
        PV(b, s)
        s = sn;
        if (b < 8) WO_PIECE(b)
        if (b < 4) WO_PIECE(8 + b)
      }
    } else {
#pragma unroll 1
      for (int e = 0; e < 12; ++e) WO_PIECE(e)
      { f32x16 s = QK(8, nm); MASK_DIAG(s); PV(8, s) }
#pragma unroll 1
      for (int b = bmin; b < 8; ++b) { f32x16 s = QK(b, nm); PV(b, s) }
    }
    const float inv = __builtin_amdgcn_rcpf(o[12]);
    const int mt = 2 * qg + (r32 >> 4);
    const int ob = WS_OF + (gi * 8) * 20 * 1024;
    __builtin_amdgcn_raw_buffer_store_b128(
        v4u{pk2(o[0] * inv, o[1] * inv), pk2(o[2] * inv, o[3] * inv), pk2(o[4] * inv, o[5] * inv), pk2(o[6] * inv, o[7] * inv)}, rs,
        mt * 20480 + ((2 * hh + h) * 16 + tok) * 16, ob + gj * 1024, AUX_SC1);
    if (h == 0)
      __builtin_amdgcn_raw_buffer_store_b64(v2u{pk2(o[8] * inv, o[9] * inv), pk2(o[10] * inv, o[11] * inv)}, rs,
          mt * 20480 + tok * 16, ob + (16 + (gj >> 2)) * 1024 + (gj & 3) * 256 + 8 * hh, AUX_SC1);
  }
  __builtin_amdgcn_s_setprio(0);
  VMWAIT(0);
  BAR();
  STAMP(5);
  const int mt3 = 4 * rh + v;
  if (isM) {
    u32 hres[10];
    if (wave == 0 && lane < 16) __hip_atomic_store(inbox + (1 * 256 + gi * 16 + lane) * 16 + gj, tag, RLX_AGENT);
    v2u xres[5];
    const GAS char* xr = (const GAS char*)P.x + (size_t)(128 * gi + 16 * mt3 + tok) * 1280 + ct * 160 + g4 * 8;
#pragma unroll
    for (int t = 0; t < 5; ++t) xres[t] = *(const GAS v2u*)(xr + t * 32);
    if (wave == 0) { poll_flags(inbox + (1 * 256 + me) * 16 + (lane & 15), lane < 16, tag); setp(L, wave, lane, 17); }
    else waitp1(L, 0, 17);
    STAMP(6);
    {
      v4u a[20];
      const int ab = WS_OF + (gi * 8 + mt3) * 20 * 1024;
#pragma unroll
      for (int s = 0; s < 20; ++s) { const int ks = KS3(s); a[s] = __builtin_amdgcn_raw_buffer_load_b128(rs, l16, ab + ks * 1024, AUX_SC1); }
      SCHED();
      setp(L, wave, lane, 18);
      STAMP(17);
      f32x4 acc[5];
#pragma unroll
      for (int n = 0; n < 5; ++n) acc[n] = zero;
      LAS char* bb = L + L_WO + l16;
      LAS char* bh = opaque(bb + 12 * 5120);
      v4u bf[3][5];
#pragma unroll
      for (int k = 0; k < 2; ++k) {
        LAS char* b = bb + k * 5120;
#pragma unroll
        for (int n = 0; n < 5; ++n) bf[k][n] = *(LAS v4u*)(b + n * 1024);
      }
#pragma unroll
      for (int s = 0; s < 20; ++s) {
        if (s == 16) waitp(L, 1, 14);
        if (s + 2 < 20) {
          LAS char* b = s + 2 < 12 ? bb + (s + 2) * 5120 : bh + (s + 2 - 12) * 5120;
#pragma unroll
          for (int n = 0; n < 5; ++n) bf[(s + 2) % 3][n] = *(LAS v4u*)(b + n * 1024);
        }
#pragma unroll
        for (int n = 0; n < 5; ++n) acc[n] = mfma(bf[s % 3][n], a[s], acc[n]);
#pragma unroll
        for (int n = 0; n < 5; ++n) { SGB(0x8, 1); SGB(0x100, 1); }
        SCHED();
      }
      setp(L, wave, lane, 19);
      STAMP(18);
      float s1 = 0.f, s2 = 0.f;
#pragma unroll
      for (int n = 0; n < 5; ++n) {
        const float h0 = bflo(xres[n].x) + rbf(acc[n][0]), h1 = bfhi(xres[n].x) + rbf(acc[n][1]);
        const float h2 = bflo(xres[n].y) + rbf(acc[n][2]), h3 = bfhi(xres[n].y) + rbf(acc[n][3]);
        hres[2 * n] = pk2(h0, h1); hres[2 * n + 1] = pk2(h2, h3);
        s1 = dot2(hres[2 * n], 0x3F803F80u, s1); s1 = dot2(hres[2 * n + 1], 0x3F803F80u, s1);
        s2 = dot2(hres[2 * n], hres[2 * n], s2); s2 = dot2(hres[2 * n + 1], hres[2 * n + 1], s2);
      }
      const int hb = WS_HF + (gi * 8 + mt3) * 20 * 1024 + ((5 * ct) >> 1) * 1024;
      if ((ct & 1) == 0) {
        __builtin_amdgcn_raw_buffer_store_b128(v4u{hres[0], hres[1], hres[2], hres[3]}, rs, l16, hb, AUX_SC1);
        __builtin_amdgcn_raw_buffer_store_b128(v4u{hres[4], hres[5], hres[6], hres[7]}, rs, l16, hb + 1024, AUX_SC1);
        __builtin_amdgcn_raw_buffer_store_b64(v2u{hres[8], hres[9]}, rs, l16, hb + 2048, AUX_SC1);
      } else {
        __builtin_amdgcn_raw_buffer_store_b64(v2u{hres[0], hres[1]}, rs, l16 + 8, hb, AUX_SC1);
        __builtin_amdgcn_raw_buffer_store_b128(v4u{hres[2], hres[3], hres[4], hres[5]}, rs, l16, hb + 1024, AUX_SC1);
        __builtin_amdgcn_raw_buffer_store_b128(v4u{hres[6], hres[7], hres[8], hres[9]}, rs, l16, hb + 2048, AUX_SC1);
      }
      s1 = sum4(s1); s2 = sum4(s2);
      if (g4 == 0)
        __builtin_amdgcn_raw_buffer_store_b64(v2u{__float_as_uint(s1), __float_as_uint(s2)}, rs,
            WS_ST + (128 * gi + 16 * mt3 + tok) * 64 + ct * 8, 0, AUX_SC1);
      setp(L, wave, lane, 20);
      VMWAIT(0);
      setp(L, wave, lane, 21);
    }
    STAMP(7);
    if (wave == 0) {
      waitp(L, 0, 21);
      if (lane < 16) __hip_atomic_store(inbox + (2 * 256 + gi * 16 + lane) * 16 + gj, tag, RLX_AGENT);
      poll_flags(inbox + (2 * 256 + me) * 16 + (lane & 15), lane < 16, tag);
      setp(L, wave, lane, 22);
    } else waitp1(L, 0, 22);
    STAMP(8);
    {
      constexpr int W = 10, W0 = 4;
      v4u a[W][2];
      const int ab = WS_HF + (gi * 8 + 2 * wave) * 20 * 1024;
#define A4LD(s) { a[(s) % W][0] = __builtin_amdgcn_raw_buffer_load_b128(rs, l16, ab + KS4(s) * 1024, AUX_SC1); \
        a[(s) % W][1] = __builtin_amdgcn_raw_buffer_load_b128(rs, l16, ab + (20 + KS4(s)) * 1024, AUX_SC1); }
#pragma unroll
      for (int s = 0; s < W0; ++s) A4LD(s)
      v4u stv[2];
#pragma unroll
      for (int m = 0; m < 2; ++m)
        stv[m] = __builtin_amdgcn_raw_buffer_load_b128(rs, WS_ST + (128 * gi + 32 * wave + 16 * m + tok) * 64 + g4 * 16, 0, AUX_SC1);
      SCHED();
      f32x4 acc[2][7];
#pragma unroll
      for (int m = 0; m < 2; ++m)
#pragma unroll
        for (int n = 0; n < 7; ++n) acc[m][n] = zero;
      LAS char* bb = L + l16;
      LAS char* bA = opaque(bb + L_W1A);
      LAS char* bC = opaque(bb + 8 * 7168);
#define W1P(p) ((p) < 8 ? bA + (p) * 7168 : ((p) < 16 ? bb + ((p) - 8) * 7168 : bC + ((p) - 16) * 7168))
      waitp(L, 1, 15);
      STAMP(19);
      v4u bf[2][7];
      {
        LAS char* b = W1P(0);
#pragma unroll
        for (int n = 0; n < 7; ++n) bf[0][n] = *(LAS v4u*)(b + n * 1024);
      }
#pragma unroll
      for (int s = 0; s < 20; ++s) {
        if (s + 1 < 20) {
          LAS char* b = W1P(s + 1);
#pragma unroll
          for (int n = 0; n < 7; ++n) bf[(s + 1) & 1][n] = *(LAS v4u*)(b + n * 1024);
        }
        const v4u a0 = a[s % W][0], a1 = a[s % W][1];
#pragma unroll
        for (int j = RAMP(s, W0, W); j < RAMP(s + 1, W0, W); ++j) if (j < 20) A4LD(j)
#pragma unroll
        for (int n = 0; n < 7; ++n) {
          acc[0][n] = mfma(bf[s & 1][n], a0, acc[0][n]);
          acc[1][n] = mfma(bf[s & 1][n], a1, acc[1][n]);
        }
#pragma unroll
        for (int n = 0; n < 7; ++n) { SGB(0x8, 2); SGB(0x100, 1); }
        SCHED();
        if (s == 10 && lane == 0) *(volatile LAS u32*)(L + L_PROG + wave * 4) = 23u;
      }
      setp(L, wave, lane, 24);
      STAMP(12);
      float mb2[2], rs2[2];
#pragma unroll
      for (int m = 0; m < 2; ++m) {
        const float t1 = sum4(__uint_as_float(stv[m].x) + __uint_as_float(stv[m].z));
        const float t2 = sum4(__uint_as_float(stv[m].y) + __uint_as_float(stv[m].w));
        const float mean = t1 * (1.f / 640.f);
        const float var = fmaxf(t2 * (1.f / 640.f) - mean * mean, 0.f);
        mb2[m] = rbf(mean);
        rs2[m] = 1.f / rbf(sqrtf(rbf(rbf(var) + 1e-5f)));
      }
      u32 hp[2][7][2];
#pragma unroll
      for (int n = 0; n < 7; ++n) {
        const f32x4 cs = *(LAS f32x4*)(L + L_CS + (16 * n + 4 * g4) * 4);
#pragma unroll
        for (int m = 0; m < 2; ++m) {
          hp[m][n][0] = pk2(fmaxf((acc[m][n][0] - mb2[m] * cs[0]) * rs2[m], 0.f), fmaxf((acc[m][n][1] - mb2[m] * cs[1]) * rs2[m], 0.f));
          hp[m][n][1] = pk2(fmaxf((acc[m][n][2] - mb2[m] * cs[2]) * rs2[m], 0.f), fmaxf((acc[m][n][3] - mb2[m] * cs[3]) * rs2[m], 0.f));
        }
      }
      const int fbs = WS_FF + ((gi * 8 + 2 * wave) * 54 + (T0 >> 1)) * 1024;
#pragma unroll
      for (int m = 0; m < 2; ++m) {
        const int fb = fbs + m * 54 * 1024;
        if ((T0 & 1) == 0) {
#pragma unroll
          for (int e = 0; e < 3; ++e)
            __builtin_amdgcn_raw_buffer_store_b128(v4u{hp[m][2 * e][0], hp[m][2 * e][1], hp[m][2 * e + 1][0], hp[m][2 * e + 1][1]}, rs, l16, fb + e * 1024, AUX_SC1);
          if (nT == 7) __builtin_amdgcn_raw_buffer_store_b64(v2u{hp[m][6][0], hp[m][6][1]}, rs, l16, fb + 3 * 1024, AUX_SC1);
        } else {
          __builtin_amdgcn_raw_buffer_store_b64(v2u{hp[m][0][0], hp[m][0][1]}, rs, l16 + 8, fb, AUX_SC1);
#pragma unroll
          for (int e = 0; e < 3; ++e)
            __builtin_amdgcn_raw_buffer_store_b128(v4u{hp[m][2 * e + 1][0], hp[m][2 * e + 1][1], hp[m][2 * e + 2][0], hp[m][2 * e + 2][1]}, rs, l16, fb + (e + 1) * 1024, AUX_SC1);
        }
      }
      VMWAIT(0);
      setp(L, wave, lane, 25);
    }
    STAMP(9);
    if (wave == 0) {
      waitp(L, 0, 25);
      if (lane < 16) __hip_atomic_store(inbox + (3 * 256 + gi * 16 + lane) * 16 + gj, tag, RLX_AGENT);
      poll_flags(inbox + (3 * 256 + me) * 16 + (lane & 15), lane < 16, tag);
      setp(L, wave, lane, 26);
    } else waitp1(L, 0, 26);
    STAMP(10);
    {
      const int mh = wave >> 1, kp = wave & 1;
      constexpr int W = 12, W0 = 4;
      v4u a[W][2];
      const int ab = WS_FF + (gi * 8 + 4 * rh + 2 * mh) * 54 * 1024;
#define A5LD(i) { const int o_ = ab + KS5(2 * (i) + kp) * 1024; a[(i) % W][0] = __builtin_amdgcn_raw_buffer_load_b128(rs, l16, o_, AUX_SC1); \
        a[(i) % W][1] = __builtin_amdgcn_raw_buffer_load_b128(rs, l16, o_ + 54 * 1024, AUX_SC1); }
#pragma unroll
      for (int i = 0; i < W0; ++i) A5LD(i)
      SCHED();
      f32x4 acc[2][5];
#pragma unroll
      for (int m = 0; m < 2; ++m)
#pragma unroll
        for (int n = 0; n < 5; ++n) acc[m][n] = zero;
      LAS char* bb = L + L_W2 + kp * 5120 + l16;
      LAS char* b1 = opaque(bb + 12 * 5120);
      LAS char* b2 = opaque(bb + 24 * 5120);
#define W2P(i) ((i) % 15 < 6 ? bb + ((i) % 15) * 10240 : ((i) % 15 < 12 ? b1 + ((i) % 15 - 6) * 10240 : b2 + ((i) % 15 - 12) * 10240))
      waitp(L, 1, 18);
      STAMP(20);
      v4u bf[2][5];
#pragma unroll
      for (int n = 0; n < 5; ++n) bf[0][n] = *(LAS v4u*)(W2P(0) + n * 1024);
#pragma unroll
      for (int i = 0; i < 27; ++i) {
        if (i == 14) waitp(L, 1, 19);
        if (i == 20) waitp(L, 1, 20);
        if (i + 1 < 27) {
#pragma unroll
          for (int n = 0; n < 5; ++n) bf[(i + 1) & 1][n] = *(LAS v4u*)(W2P(i + 1) + n * 1024);
        }
        const v4u a0 = a[i % W][0], a1 = a[i % W][1];
#pragma unroll
        for (int j = RAMP(i, W0, W); j < RAMP(i + 1, W0, W); ++j) if (j < 27) A5LD(j)
#pragma unroll
        for (int n = 0; n < 5; ++n) {
          acc[0][n] = mfma(bf[i & 1][n], a0, acc[0][n]);
          acc[1][n] = mfma(bf[i & 1][n], a1, acc[1][n]);
        }
#pragma unroll
        for (int n = 0; n < 5; ++n) { SGB(0x8, 2); SGB(0x100, 1); }
        SCHED();
        if (i == 5) setp(L, wave, lane, 27);
        if (i == 11) setp(L, wave, lane, 28);
        if (i == 15) setp(L, wave, lane, 29);
      }
      LAS char* xs = L + L_W2 + 24 * 5120 + l16;
      waitp(L, 0, 29);
#pragma unroll
      for (int n = 0; n < 5; ++n) *(LAS f32x4*)(xs + (wave ^ 1) * 5120 + n * 1024) = kp ? acc[0][n] : acc[1][n];
      setp(L, wave, lane, 30);
      waitp1(L, wave ^ 1, 30);
      GAS char* orow = (GAS char*)P.out + (size_t)(128 * gi + 16 * mt3 + tok) * 1280 + ct * 160 + g4 * 8;
#pragma unroll
      for (int n = 0; n < 5; ++n) {
        const f32x4 t = (kp ? acc[1][n] : acc[0][n]) + *(LAS f32x4*)(xs + wave * 5120 + n * 1024);
        v2u o;
        o.x = pk2(bflo(hres[2 * n]) + rbf(t[0]), bfhi(hres[2 * n]) + rbf(t[1]));
        o.y = pk2(bflo(hres[2 * n + 1]) + rbf(t[2]), bfhi(hres[2 * n + 1]) + rbf(t[3]));
        *(GAS v2u*)(orow + n * 32) = o;
      }
    }
  } else {
    {
#pragma unroll
      for (int e = 0; e < 3; ++e) { int u = 90 + v + 4 * e; u = u > 99 ? 99 : u; const int ks = u / 5; dmas(rsi, l16, WS_WOT + ct * 5120 + ks * 40960 + (u - 5 * ks) * 1024, lds0 + L_WO + u * 1024); }
    }
#pragma unroll
    for (int e = 0; e < 14; ++e) { int u = v + 4 * e + 28 * (gi & 1); u = u >= 56 ? u - 56 : u; const int ks = u / 7; dmas(rsi, l16, WS_W1T + (KS4(ks) * 108 + T0 + (u - 7 * ks)) * 1024, lds0 + L_W1A + u * 1024); }
    VMWAIT(14);
    setp(L, wave, lane, 14);
    if (wave == 7) {
      v4u cp[20];
#pragma unroll
      for (int k = 0; k < 20; ++k) cp[k] = __builtin_amdgcn_raw_buffer_load_b128(rs, (lane < 28 ? lane : 0) * 16, WS_CS1 + (k * 1792 + 16 * T0) * 4, AUX_SC1);
      f32x4 c4 = zero;
#pragma unroll
      for (int k = 0; k < 20; ++k) c4 += __builtin_bit_cast(f32x4, cp[k]);
      if (lane < 28) *(LAS f32x4*)(L + L_CS + lane * 16) = c4;
    }
    STAMP(6);
#define W2LD(u) __builtin_amdgcn_raw_buffer_load_b128(rs, l16, WS_W2T + (KS5((u) / 5) * 40 + 5 * ct + ((u) - 5 * ((u) / 5))) * 1024, AUX_SC1)
    const int w2rot = 2 * (gi & 1) + rh;
    waitps(L, 0, 18);
    {
      v4u pre[21];
#pragma unroll
      for (int e = 0; e < 21; ++e) { int u = v + 4 * e + 42 * (gi & 1); u = u >= 84 ? u - 84 : u; const int ks = u / 7;
        pre[e] = __builtin_amdgcn_raw_buffer_load_b128(rs, l16, WS_W1T + (KS4(8 + ks) * 108 + T0 + (u - 7 * ks)) * 1024, AUX_SC1); }
      SCHED();
      waitp(L, 0, 19);
#pragma unroll
      for (int e = 0; e < 21; ++e) { int u = v + 4 * e + 42 * (gi & 1); u = u >= 84 ? u - 84 : u; *(LAS v4u*)(L + L_W1B + u * 1024 + l16) = pre[e]; }
    }
    setp(L, wave, lane, 15);
    STAMP(7);
    v4u wb[20], wc[25];
#define ROT(x, n, r) ((x) + (r) >= (n) ? (x) + (r) - (n) : (x) + (r))
    waitp(L, 0, 21);
#pragma unroll
    for (int e = 0; e < 20; ++e) { const int u = ROT(v + 4 * e, 80, 20 * w2rot); wb[e] = W2LD(u); }
    SCHED();
    STAMP(8);
    waitps(L, 0, 23);
#pragma unroll
    for (int e = 0; e < 25; ++e) { const int u = 80 + ROT(v + 4 * e, 100, 0); wc[e] = W2LD(u); }
    SCHED();
    waitp(L, 0, 24);
#pragma unroll
    for (int e = 0; e < 20; ++e) { const int u = ROT(v + 4 * e, 80, 20 * w2rot); *(LAS v4u*)(L + L_W2 + u * 1024 + l16) = wb[e]; }
#pragma unroll
    for (int e = 0; e < 18; ++e) { const int u = 80 + v + 4 * e; if (u < 150) *(LAS v4u*)(L + L_W2 + u * 1024 + l16) = wc[e]; }
    setp(L, wave, lane, 18);
    STAMP(10);
    v4u wd[23];
    waitp(L, 0, 25);
#pragma unroll
    for (int e = 0; e < 23; ++e) { int u = 180 + v + 4 * e; u = u > 269 ? 269 : u; wd[e] = W2LD(u); }
    SCHED();
#define W2PUT(u, val) { *(LAS v4u*)(L + L_W2 + ((u) - 150) * 1024 + l16) = (val); }
    waitp(L, 0, 27);
#pragma unroll
    for (int e = 17; e < 25; ++e) { const int u = 80 + v + 4 * e; if (u >= 150) W2PUT(u, wc[e]) }
#pragma unroll
    for (int e = 0; e < 8; ++e) { const int u = 180 + v + 4 * e; if (u < 210) W2PUT(u, wd[e]) }
    setp(L, wave, lane, 19);
    waitp(L, 0, 28);
#pragma unroll
    for (int e = 7; e < 23; ++e) { const int u = 180 + v + 4 * e; if (u >= 210 && u < 270) W2PUT(u, wd[e]) }
    setp(L, wave, lane, 20);
  }
  STAMP(11);
  asm volatile("fb_code_end:");
}
}

extern "C" void kernel_launch(void* const* d_in, const int* in_sizes, int n_in, void* d_out, int out_size, void* d_ws,
                              size_t ws_size, hipStream_t stream) {
  fb::Params P;
  P.x = (const char*)d_in[0]; P.wqkv = (const char*)d_in[1]; P.wo = (const char*)d_in[2];
  P.w1 = (const char*)d_in[4]; P.w2 = (const char*)d_in[5]; P.out = (char*)d_out; P.ws = (char*)d_ws;
  (void)hipFuncSetAttribute((const void*)fb::fused_block, hipFuncAttributeMaxDynamicSharedMemorySize, fb::LDS_BYTES);
  fb::fused_block<<<256, 512, fb::LDS_BYTES, stream>>>(P);
}
```

```cpp
#include <hip/hip_runtime.h>
#include <hip/hip_bf16.h>
#include <stdint.h>

typedef unsigned short u16;
typedef unsigned int u32;
typedef unsigned long long u64;
typedef u32 v4u __attribute__((ext_vector_type(4)));
typedef u32 v2u __attribute__((ext_vector_type(2)));
typedef float f32x4 __attribute__((ext_vector_type(4)));
typedef float f32x2 __attribute__((ext_vector_type(2)));
typedef float f32x16 __attribute__((ext_vector_type(16)));
typedef short s16x4 __attribute__((ext_vector_type(4)));
typedef __bf16 bf16x8 __attribute__((ext_vector_type(8)));
typedef __bf16 bf16x2 __attribute__((ext_vector_type(2)));
#define LAS __attribute__((address_space(3)))
#define GAS __attribute__((address_space(1)))
#define DI __device__ __forceinline__
#define RLX_AGENT __ATOMIC_RELAXED, __HIP_MEMORY_SCOPE_AGENT

#define STAMPS 0
#define CODE_PF 1

namespace fb {
constexpr int WS_EPOCH = 0;
constexpr int WS_FLAGS = 4096;
constexpr int WS_INBOX = 16384;
constexpr int WS_IMG = WS_INBOX + 4 * 256 * 128;
constexpr int WS_OF = WS_IMG + 3 * 128 * 20480;
constexpr int WS_HF = WS_OF + 16 * 8 * 20 * 1024;
constexpr int WS_ST = WS_HF + 16 * 8 * 20 * 1024;
constexpr int WS_FF = WS_ST + 2048 * 64;
constexpr int WS_WOT = WS_FF + 16 * 8 * 54 * 1024;
constexpr int WS_W1T = WS_WOT + 20 * 40 * 1024;
constexpr int WS_W2T = WS_W1T + 20 * 108 * 1024;
constexpr int WS_CS1 = WS_W2T + 54 * 40 * 1024;
constexpr int WS_ST0 = WS_CS1 + 20 * 1792 * 4;
constexpr int WS_END = WS_ST0 + 2048 * 8;
constexpr int WS_STAMP = WS_END;
constexpr int LDS_BYTES = 163840;
constexpr int L_RING = 0;
constexpr int L_XR = 81920;
constexpr int L_UNIT = 147456;
constexpr int L_ST1 = 159744, L_EP = 161792;
constexpr int L_TAB = 162816;
constexpr int L_CS = L_TAB, L_PROG = L_TAB + 512;
constexpr int L_WO = 0;
constexpr int L_K = 92160;
constexpr int L_V = L_K + 24 * 1280, V_TS = 1616;
constexpr int L_W1A = 102400;
constexpr int L_W1B = 0;
constexpr int L_W2 = 0;
constexpr int AUX_SC1 = 16;
constexpr u32 SPIN_CAP = 1u << 17;

DI f32x4 mfma(v4u a, v4u b, f32x4 c) {
  return __builtin_amdgcn_mfma_f32_16x16x32_bf16(__builtin_bit_cast(bf16x8, a), __builtin_bit_cast(bf16x8, b), c, 0, 0, 0);
}
DI f32x16 mfma32(v4u a, v4u b, f32x16 c) {
  return __builtin_amdgcn_mfma_f32_32x32x16_bf16(__builtin_bit_cast(bf16x8, a), __builtin_bit_cast(bf16x8, b), c, 0, 0, 0);
}
DI float max3(float a, float b, float c) { return __builtin_fmaxf(__builtin_fmaxf(a, b), c); }
DI v2u trr(LAS char* p) { return __builtin_bit_cast(v2u, __builtin_amdgcn_ds_read_tr16_b64_v4i16((LAS s16x4*)p)); }
DI v4u trfrag(LAS char* p0, LAS char* p1) { v2u a = trr(p0), b = trr(p1); return v4u{a.x, a.y, b.x, b.y}; }
DI u32 pk2(float a, float b) { f32x2 v = {a, b}; return __builtin_bit_cast(u32, __builtin_convertvector(v, bf16x2)); }
DI float bflo(u32 u) { return __uint_as_float(u << 16); }
DI float bfhi(u32 u) { return __uint_as_float(u & 0xFFFF0000u); }
DI float rbf(float f) { return bflo(pk2(f, 0.f)); }
DI float dot2(u32 a, u32 b, float c) {
  return __builtin_amdgcn_fdot2_f32_bf16(__builtin_bit_cast(bf16x2, a), __builtin_bit_cast(bf16x2, b), c, false);
}
DI float sum4(float v) {
  auto r = __builtin_amdgcn_permlane16_swap(__float_as_uint(v), __float_as_uint(v), false, false);
  v = __uint_as_float(r[0]) + __uint_as_float(r[1]);
  auto s = __builtin_amdgcn_permlane32_swap(__float_as_uint(v), __float_as_uint(v), false, false);
  return __uint_as_float(s[0]) + __uint_as_float(s[1]);
}
DI float max4(float v) {
  auto r = __builtin_amdgcn_permlane16_swap(__float_as_uint(v), __float_as_uint(v), false, false);
  v = fmaxf(__uint_as_float(r[0]), __uint_as_float(r[1]));
  auto s = __builtin_amdgcn_permlane32_swap(__float_as_uint(v), __float_as_uint(v), false, false);
  return fmaxf(__uint_as_float(s[0]), __uint_as_float(s[1]));
}
DI void dma16(const void* sbase, u32 voff, u32 lds_dst) {
  u32 keep;
  asm volatile("s_mov_b32 %0, m0\n\ts_mov_b32 m0, %3\n\ts_nop 0\n\tglobal_load_lds_dwordx4 %1, %2\n\ts_mov_b32 m0, %0"
               : "=&s"(keep) : "v"(voff), "s"(sbase), "s"(__builtin_amdgcn_readfirstlane(lds_dst)) : "memory");
}
typedef int v4i __attribute__((ext_vector_type(4)));
DI v4i mkrsrc(const void* p, int bytes) { return v4i{(int)(u32)(uintptr_t)p, (int)((uintptr_t)p >> 32), bytes, 0x00020000}; }
DI void dmab(v4i rsrc, u32 voff, u32 soff, u32 lds_dst) {
  asm volatile("s_mov_b32 m0, %3\n\ts_nop 0\n\tbuffer_load_dwordx4 %0, %1, %2 offen lds"
               :: "v"(voff), "s"(rsrc), "s"(__builtin_amdgcn_readfirstlane(soff)), "s"(__builtin_amdgcn_readfirstlane(lds_dst)) : "memory", "m0");
}
DI void dmas(v4i rsrc, u32 voff, u32 soff, u32 lds_dst) {
  asm volatile("s_mov_b32 m0, %3\n\ts_nop 0\n\tbuffer_load_dwordx4 %0, %1, %2 offen sc1 lds"
               :: "v"(voff), "s"(rsrc), "s"(__builtin_amdgcn_readfirstlane(soff)), "s"(__builtin_amdgcn_readfirstlane(lds_dst)) : "memory", "m0");
}
DI void dmas_nc(v4i rsrc, u32 voff, u32 soff, u32 lds_dst) {
  asm volatile("s_mov_b32 m0, %3\n\ts_nop 0\n\tbuffer_load_dwordx4 %0, %1, %2 offen sc1 lds"
               :: "v"(voff), "s"(rsrc), "s"(__builtin_amdgcn_readfirstlane(soff)), "s"(__builtin_amdgcn_readfirstlane(lds_dst)) : "m0");
}
#define VMWAIT(n) asm volatile("s_waitcnt vmcnt(" #n ")" ::: "memory")
#define BAR() do { asm volatile("s_waitcnt lgkmcnt(0)" ::: "memory"); __builtin_amdgcn_s_barrier(); asm volatile("" ::: "memory"); } while (0)
#define SCHED() __builtin_amdgcn_sched_barrier(0)
#define SGB(mask, n) __builtin_amdgcn_sched_group_barrier(mask, n, 0)
DI LAS char* opaque(LAS char* p) { asm volatile("" : "+v"(p)); return p; }

DI u64 mix64(u64 z) {
  z += 0x9E3779B97F4A7C15ull; z = (z ^ (z >> 30)) * 0xBF58476D1CE4E5B9ull; z = (z ^ (z >> 27)) * 0x94D049BB133111EBull;
  return z ^ (z >> 31);
}
DI void poll_flags(GAS u64* p, bool active, u64 tag) {
  for (u32 spins = 0; spins < SPIN_CAP; ++spins) {
    u64 v = tag;
    if (active) v = __hip_atomic_load(p, RLX_AGENT);
    if (__all(v == tag)) break;
    __builtin_amdgcn_s_sleep(1);
  }
  __builtin_amdgcn_fence(__ATOMIC_ACQUIRE, "wavefront");
}

DI void setp(LAS char* L, int wave, int lane, u32 step) {
  asm volatile("s_waitcnt lgkmcnt(0)" ::: "memory");
  if (lane == 0) *(volatile LAS u32*)(L + L_PROG + wave * 4) = step;
  asm volatile("" ::: "memory");
}
DI void waitp(LAS char* L, int grp, u32 step) {
  for (u32 spins = 0; spins < (1u << 24); ++spins) {
    const v4u x = *(volatile LAS v4u*)(L + L_PROG + grp * 16);
    const u32 m = min(min(x.x, x.y), min(x.z, x.w));
    if (__builtin_amdgcn_readfirstlane(m) >= step) break;
  }
  asm volatile("" ::: "memory");
}
DI void waitps(LAS char* L, int grp, u32 step) {
  for (u32 spins = 0; spins < (1u << 22); ++spins) {
    const v4u x = *(volatile LAS v4u*)(L + L_PROG + grp * 16);
    const u32 m = min(min(x.x, x.y), min(x.z, x.w));
    if (__builtin_amdgcn_readfirstlane(m) >= step) break;
    __builtin_amdgcn_s_sleep(2);
  }
  asm volatile("" ::: "memory");
}
DI void waitp2(LAS char* L, int w, u32 step) {
  for (u32 spins = 0; spins < (1u << 24); ++spins) {
    const v2u x = *(volatile LAS v2u*)(L + L_PROG + w * 4);
    if (__builtin_amdgcn_readfirstlane(min(x.x, x.y)) >= step) break;
  }
  asm volatile("" ::: "memory");
}
DI void waitp1(LAS char* L, int w, u32 step) {
  for (u32 spins = 0; spins < (1u << 24); ++spins) {
    const u32 x = *(volatile LAS u32*)(L + L_PROG + w * 4);
    if (__builtin_amdgcn_readfirstlane(x) >= step) break;
  }
  asm volatile("" ::: "memory");
}

struct Params {
  const char* x; const char* wqkv; const char* wo; const char* w1; const char* w2; char* out; char* ws;
};

#define STAMP(k) do {} while (0)

#define LST(k)
#define RAMP(i, W0, W) ((W0) + 2 * (i) < (i) + (W) - 1 ? (W0) + 2 * (i) : (i) + (W) - 1)
__global__ void __launch_bounds__(512, 2) fused_block(Params P) {
  extern __shared__ __attribute__((aligned(16))) char smem[];
  LAS char* L = (LAS char*)smem;
  const u32 lds0 = (u32)(uintptr_t)L;
  const int tid = threadIdx.x, lane = tid & 63;
  const int wave = __builtin_amdgcn_readfirstlane(tid >> 6);
  const int bid = blockIdx.x;
  const int gi = (bid & 7) * 2 + (bid >> 7);
  const int gj = (bid >> 3) & 15;
  const int tok = lane & 15, g4 = lane >> 4, q4 = (lane & 15) >> 2, p4 = lane & 3;
  const bool isM = wave < 4;
  const int v = wave & 3;
  const __amdgpu_buffer_rsrc_t rs = __builtin_amdgcn_make_buffer_rsrc((void*)P.ws, 0, WS_END, 0x00020000);
  GAS u64* flags = (GAS u64*)(P.ws + WS_FLAGS);
  GAS u64* inbox = (GAS u64*)(P.ws + WS_INBOX);
  const int me = gi * 16 + gj;
#define P1MAP 1
  const int ti = ((bid & 7) >> 1) * 4 + ((bid >> 3) & 3), tj = (bid & 1) * 8 + (bid >> 5);
#define ROTM 2
  const int rot = ROTM == 0 ? 0 : (ROTM == 1 ? ((ti + tj) & 1) * 5 : (ti + tj) % 10);
  const int l16 = lane * 16;
  const bool isX = wave >= 6;
  const int lv = wave & 1, D = isX ? 4 : 5;
  const v4i rl = isX ? mkrsrc(P.x, 2048 * 1280) : mkrsrc(P.wqkv, 640 * 3840);
  u32 vo[2];
#pragma unroll
  for (int k = 0; k < 2; ++k)
    vo[k] = isX ? (lane >> 3) * 1280 + (((lane & 7) ^ (lane >> 3)) * 16) : g4 * 3840 + (((tok >> 1) ^ (g4 | (k << 2))) * 32) + (tok & 1) * 16;
  const u32 sb = isX ? (128 * ti + 64 * lv) * 1280 : tj * 256 + (32 * lv) * 3840;
  const u32 cstr = isX ? 128 : 64 * 3840, pstr = isX ? 8 * 1280 : 4 * 3840;
  const u32 db = lds0 + (isX ? L_XR : L_RING) + lv * 8192;
  int slot = 0, kc = rot;
#define L_ISSUE() { _Pragma("unroll") for (int e = 0; e < 8; ++e) dmab(rl, vo[(e >> 1) & 1], sb + kc * cstr + e * pstr, db + slot * 16384 + e * 1024); \
    slot = slot + 1 == D ? 0 : slot + 1; kc = kc == 9 ? 0 : kc + 1; }
  if (!isM && tj != 15) L_ISSUE()
  const int rh = gj >> 3, ct = gj & 7;
  const int T0 = gj < 12 ? 7 * gj : 84 + 6 * (gj - 12);
  const int nT = gj < 12 ? 7 : 6;
  const int r5 = (ct * 30) >> 3;
  const int r3 = 2 * ct, r4 = (gj * 5) >> 2;
#define KS3(p) ((p) < 16 ? (((p) + r3) & 15) : (p))
#define KS4(s) ((s) + r4 >= 20 ? (s) + r4 - 20 : (s) + r4)
#define KS5(s) ((s) < 30 ? ((s) + r5 >= 30 ? (s) + r5 - 30 : (s) + r5) : (s))
  const v4u ones = {0x3F803F80u, 0x3F803F80u, 0x3F803F80u, 0x3F803F80u};
  const f32x4 zero = {0.f, 0.f, 0.f, 0.f};
  STAMP(0);
  {
    u64 pc0; u32 len;
    asm volatile("s_getpc_b64 %0\n\ts_mov_b32 %1, fb_code_end-." : "=s"(pc0), "=s"(len));
    if (wave == CODE_PF && len <= 131072u) {
      const u32 per = ((len >> 5) + 1023u) & ~1023u;
      const v4i rc = mkrsrc((const void*)(pc0 & ~63ull), (int)len);
      for (u32 o = (bid >> 3) * per; o < ((bid >> 3) + 1) * per; o += 1024) dmab(rc, o + l16, 0, lds0 + L_ST1);
    }
  }
  if (tid < 8) *(volatile LAS u32*)(L + L_PROG + tid * 4) = 0u;
  const v4i rsi = mkrsrc(P.ws, WS_END);
  if (wave == 0) dmas(rsi, l16, 0, lds0 + L_EP);
  u64 tag;
#define GET_TAG() { const v2u ep = *(volatile LAS v2u*)(L + L_EP); \
    tag = mix64((u64)__builtin_amdgcn_readfirstlane(ep.x) | ((u64)__builtin_amdgcn_readfirstlane(ep.y) << 32)); }
#define MAKE_TAG() { VMWAIT(0); GET_TAG() }
  v4u xv[10];
  if (wave == 0) {
    const GAS char* xr = (const GAS char*)P.x + (size_t)(128 * gi + 8 * gj + (lane >> 3)) * 1280 + (lane & 7) * 16;
#pragma unroll
    for (int e = 0; e < 10; ++e) xv[e] = *(const GAS v4u*)(xr + e * 128);
    SCHED();
  }
#define ROW_STATS() if (wave == 0) { \
    float s1 = 0.f, s2 = 0.f; \
    _Pragma("unroll") for (int e = 0; e < 10; ++e) _Pragma("unroll") for (int k = 0; k < 4; ++k) { s1 = dot2(xv[e][k], 0x3F803F80u, s1); s2 = dot2(xv[e][k], xv[e][k], s2); } \
    _Pragma("unroll") for (int o = 1; o < 8; o <<= 1) { s1 += __shfl_xor(s1, o); s2 += __shfl_xor(s2, o); } \
    const float mean = s1 * (1.f / 640.f); \
    const float var = fmaxf(s2 * (1.f / 640.f) - mean * mean, 0.f); \
    const float rstd = 1.f / rbf(sqrtf(rbf(rbf(var) + 1e-5f))); \
    if ((lane & 7) == 0) \
      __builtin_amdgcn_raw_buffer_store_b64(v2u{__float_as_uint(rbf(mean)), __float_as_uint(rstd)}, rs, (lane >> 3) * 8, WS_ST0 + (128 * gi + 8 * gj) * 8, AUX_SC1); }
#define ROW_STATS_FLAG() if (wave == 0) { MAKE_TAG() if (lane == 0) __hip_atomic_store(flags + (5 * 16 + gi) * 16 + gj, tag, RLX_AGENT); }
  bool f1act; GAS u64* f1dst;
  {
    const int sec = tj / 5, jj = tj - 5 * sec, a = lane >> 2, g = (32 * jj) / 10 + (lane & 3), ci = ti + a;
    f1act = lane < 12 && tj != 15 && ci < 16 && (sec != 0 || a == 0);
      const int slot = 2 * (sec == 0 ? 0 : (sec == 1 ? 3 : 6) - a) + (((10 * g) >> 5) == jj ? 0 : 1);
    f1dst = f1act ? inbox + (ci * 16 + g) * 16 + slot : flags + ti * 16 + tj;
    asm volatile("" : "+v"(f1dst));
  }
  if (tj == 15) {
    const int R = ti * 8 + wave;
    LAS char* stg = L + wave * 4096;
    const int srow = lane >> 3, sc = (lane & 7) * 16;
    LAS char* tb = stg + (4 * g4 + q4) * 128 + p4 * 8;
    v4u d[3][4];
    const __amdgpu_buffer_rsrc_t rwo = __builtin_amdgcn_make_buffer_rsrc((void*)P.wo, 0, 640 * 1280, 0x00020000);
    const __amdgpu_buffer_rsrc_t rw1 = __builtin_amdgcn_make_buffer_rsrc((void*)P.w1, 0, 640 * 3456, 0x00020000);
#pragma unroll
    for (int u = 0; u < 3; ++u) {
      const int U = R + 128 * u;
      if (u < 2 && U < 200) {
        const int ks = U / 10, lg = U - ks * 10;
#pragma unroll
        for (int e = 0; e < 4; ++e) {
          const int rho = 8 * e + srow, t = rho >> 4, gg = (rho >> 2) & 3, qq = rho & 3;
          const int row = ks < 16 ? (2 * ks + (gg >> 1)) * 20 + 8 * t + 4 * (gg & 1) + qq : (2 * (4 * (ks - 16) + gg) + t) * 20 + 16 + qq;
          d[u][e] = __builtin_amdgcn_raw_buffer_load_b128(rwo, row * 1280 + sc, lg * 128, 0);
        }
      } else {
        const int U1 = U - 200, ks = U1 / 27, cg = U1 - ks * 27;
#pragma unroll
        for (int e = 0; e < 4; ++e) d[u][e] = __builtin_amdgcn_raw_buffer_load_b128(rw1, (8 * e + srow) * 3456 + sc, ks * (32 * 3456) + cg * 128, 0);
      }
    }
    SCHED();
    BAR();
    ROW_STATS()
    ROW_STATS_FLAG()
#pragma unroll
    for (int u = 0; u < 3; ++u) {
      const int U = R + 128 * u;
#pragma unroll
      for (int e = 0; e < 4; ++e) *(LAS v4u*)(stg + e * 1024 + l16) = d[u][e];
      if (u < 2 && U < 200) {
        const int ks = U / 10, lg = U - ks * 10;
#pragma unroll
        for (int n = 0; n < 4; ++n)
          __builtin_amdgcn_raw_buffer_store_b128(trfrag(tb + n * 32, tb + n * 32 + 2048), rs, l16, WS_WOT + (ks * 40 + 4 * lg + n) * 1024, AUX_SC1);
      } else {
        const int U1 = U - 200, ks = U1 / 27, cg = U1 - ks * 27;
        f32x4 c4[4];
#pragma unroll
        for (int n = 0; n < 4; ++n) {
          const v4u f = trfrag(tb + n * 32, tb + n * 32 + 2048);
          __builtin_amdgcn_raw_buffer_store_b128(f, rs, l16, WS_W1T + (ks * 108 + 4 * cg + n) * 1024, AUX_SC1);
          c4[n] = mfma(f, ones, zero);
        }
        const f32x4 cv = tok == 0 ? c4[0] : (tok == 1 ? c4[1] : (tok == 2 ? c4[2] : c4[3]));
        if (tok < 4) __builtin_amdgcn_raw_buffer_store_b128(__builtin_bit_cast(v4u, cv), rs, tok * 64 + g4 * 16, WS_CS1 + (ks * 1792 + cg * 64) * 4, AUX_SC1);
      }
      if (u == 1) {
        VMWAIT(0);
        BAR();
        GET_TAG()
        if (tid == 0) __hip_atomic_store(flags + (4 * 16 + 0) * 16 + ti, tag, RLX_AGENT);
      }
    }
    VMWAIT(0);
    STAMP(1);
    BAR();
    if (tid == 0) __hip_atomic_store(flags + (4 * 16 + 1) * 16 + ti, tag, RLX_AGENT);
    if (isM) setp(L, wave, lane, 13);
  } else if (isM) {
    BAR();
    ROW_STATS()
    const int mh = wave >> 1, nh = wave & 1;
    f32x4 acc1[4][4];
#pragma unroll
    for (int m = 0; m < 4; ++m)
#pragma unroll
      for (int n = 0; n < 4; ++n) acc1[m][n] = zero;
    f32x4 cs[2] = {zero, zero};
    LAS char* bn[4];
#pragma unroll
    for (int n = 0; n < 4; ++n) bn[n] = L + L_RING + (8 * g4 + q4) * 256 + (((4 * nh + (n ^ (2 * mh))) ^ (q4 | ((g4 & 1) << 2))) * 32) + p4 * 8;
    LAS char* an[2];
#pragma unroll
    for (int k = 0; k < 2; ++k) an[k] = L + L_XR + (64 * mh + tok) * 128 + (((4 * k + g4) ^ (tok & 7)) * 16);
    v4u b[2][4], a[2][4], pg;
    u64 sfl; v2u stv[4];
    waitp(L, 1, 1);
#pragma unroll
    for (int n = 0; n < 4; ++n) { b[0][n] = trfrag(bn[n], bn[n] + 1024); a[0][n] = *(LAS v4u*)(an[0] + n * 2048); }
#pragma unroll
    for (int s = 0; s < 20; ++s) {
      if (s + 1 < 20) {
        const int c1 = (s + 1) >> 1, k1 = (s + 1) & 1, sl = c1 % 5;
        if (k1 == 1) pg = *(volatile LAS v4u*)(L + L_PROG + 16);
        if (k1 == 0) {
          if (__builtin_amdgcn_readfirstlane(min(min(pg.x, pg.y), min(pg.z, pg.w))) < (u32)(c1 + 1)) waitp(L, 1, c1 + 1);
          asm volatile("" ::: "memory");
          if (sl == 4 || sl == 0) {
#pragma unroll
            for (int n = 0; n < 4; ++n) bn[n] = opaque(bn[n] + (sl == 4 ? 65536 : -65536));
          }
        }
        const int so = (sl & 3) * 16384 + k1 * 8192;
#pragma unroll
        for (int n = 0; n < 4; ++n) {
          b[(s + 1) & 1][n] = trfrag(bn[n] + so, bn[n] + so + 1024);
          a[(s + 1) & 1][n] = *(LAS v4u*)(an[k1] + (c1 & 3) * 16384 + n * 2048);
        }
      }
#pragma unroll
      for (int n = 0; n < 4; ++n)
#pragma unroll
        for (int m = 0; m < 4; ++m) acc1[m][n] = mfma(b[s & 1][n], a[s & 1][m], acc1[m][n]);
      cs[0] = mfma(b[s & 1][0], ones, cs[0]); cs[1] = mfma(b[s & 1][1], ones, cs[1]);
#pragma unroll
      for (int n = 0; n < 13; ++n) { SGB(0x8, 1); SGB(0x100, 1); }
      SCHED();
      if ((s & 1) == 1 && s < 19 && lane == 0) *(volatile LAS u32*)(L + L_PROG + wave * 4) = (u32)(s >> 1) + 1u;
      if (s == 3) ROW_STATS_FLAG()
      if (s == 11) sfl = __hip_atomic_load(flags + (5 * 16 + ti) * 16 + tok, RLX_AGENT);
      if (s == 15) {
        GET_TAG()
        if (!__all(sfl == tag)) poll_flags(flags + (5 * 16 + ti) * 16 + tok, true, tag);
        __builtin_amdgcn_fence(__ATOMIC_ACQUIRE, "wavefront");
#pragma unroll
        for (int m = 0; m < 4; ++m) stv[m] = __builtin_amdgcn_raw_buffer_load_b64(rs, (16 * m + tok) * 8, WS_ST0 + (128 * ti + 64 * mh) * 8, AUX_SC1);
      }
    }
    if (tok == 0) {
      *(LAS f32x4*)(L + L_CS + (64 * nh + 32 * mh + 4 * g4) * 4) = cs[0];
      *(LAS f32x4*)(L + L_CS + (64 * nh + 32 * mh + 16 + 4 * g4) * 4) = cs[1];
    }
    setp(L, wave, lane, 10);
    STAMP(1);
    const int sec = tj / 5, jj = tj - 5 * sec;
    const float qs = sec == 0 ? 0.22360679774997896f * 1.4426950408889634f : 1.f;
    float mb[4], rstd[4];
#pragma unroll
    for (int m = 0; m < 4; ++m) {
      mb[m] = __uint_as_float(stv[m].x); rstd[m] = __uint_as_float(stv[m].y) * qs;
    }
    waitp(L, 0, 10);
    const int so = WS_IMG + ((sec * 128 + 8 * ti + 4 * mh) * 80 + 16 * jj) * 256;
    const int vo = (2 * (g4 & 1) + (g4 >> 1)) * 256 + tok * 16;
#pragma unroll
    for (int e = 0; e < 2; ++e) {
      const int tp = 2 * nh + (e ^ mh);
      const f32x4 c0 = *(LAS f32x4*)(L + L_CS + (32 * tp + 4 * g4) * 4), c1 = *(LAS f32x4*)(L + L_CS + (32 * tp + 16 + 4 * g4) * 4);
#pragma unroll
      for (int m = 0; m < 4; ++m) {
        const f32x4 x = acc1[m][2 * e], y = acc1[m][2 * e + 1];
        const u32 x0 = pk2((x[0] - mb[m] * c0[0]) * rstd[m], (x[1] - mb[m] * c0[1]) * rstd[m]);
        const u32 x1 = pk2((x[2] - mb[m] * c0[2]) * rstd[m], (x[3] - mb[m] * c0[3]) * rstd[m]);
        const u32 y0 = pk2((y[0] - mb[m] * c1[0]) * rstd[m], (y[1] - mb[m] * c1[1]) * rstd[m]);
        const u32 y1 = pk2((y[2] - mb[m] * c1[2]) * rstd[m], (y[3] - mb[m] * c1[3]) * rstd[m]);
        auto r0 = __builtin_amdgcn_permlane16_swap(x0, y0, false, false);
        auto r1 = __builtin_amdgcn_permlane16_swap(x1, y1, false, false);
        __builtin_amdgcn_raw_buffer_store_b128(v4u{r0[0], r1[0], r0[1], r1[1]}, rs, vo, so + m * 20480 + tp * 1024, AUX_SC1);
      }
    }
    STAMP(14);
    VMWAIT(0);
    setp(L, wave, lane, 13);
  } else {
    BAR();
#pragma unroll 1
    for (int c = 1; c < 10; ++c) {
      if (c >= D) waitp(L, 0, c - D + 1);
      L_ISSUE()
      if (c < 9) { VMWAIT(8); if (lane == 0) *(volatile LAS u32*)(L + L_PROG + wave * 4) = (u32)c; }
    }
    const int U = (ti * 15 + tj) * 4 + v;
    const bool isW2 = U < 540, hasU = U < 896;
    const int uks = isW2 ? U / 10 : (hasU ? (U - 356) / 27 : 0), ulg = isW2 ? U - uks * 10 : (hasU ? (U - 356) - uks * 27 : 0);
    {
      const v4i ru = isW2 ? mkrsrc(P.w2, 1728 * 1280) : mkrsrc(P.w1, 640 * 3456);
      const int rstr = isW2 ? 1280 : 3456;
      const u32 sd = lds0 + (v < 3 ? L_UNIT + v * 4096 : L_XR + 2 * 16384);
      if (v == 3) waitp(L, 0, 7);
#pragma unroll
      for (int e = 0; e < 4; ++e) dmab(ru, (8 * e + (lane >> 3)) * rstr + (lane & 7) * 16, uks * 32 * rstr + ulg * 128, sd + e * 1024);
      VMWAIT(12); if (lane == 0) *(volatile LAS u32*)(L + L_PROG + wave * 4) = 9u;
      VMWAIT(4); if (lane == 0) *(volatile LAS u32*)(L + L_PROG + wave * 4) = 10u;
      VMWAIT(0);
      if (hasU) {
        LAS char* tb = L + (sd - lds0) + (4 * g4 + q4) * 128 + p4 * 8;
        const int dst = isW2 ? WS_W2T + (uks * 40 + 4 * ulg) * 1024 : WS_W1T + (uks * 108 + 4 * ulg) * 1024;
        f32x4 c4[4];
#pragma unroll
        for (int n = 0; n < 4; ++n) {
          const v4u f = trfrag(tb + n * 32, tb + n * 32 + 2048);
          __builtin_amdgcn_raw_buffer_store_b128(f, rs, l16, dst + n * 1024, AUX_SC1);
          c4[n] = mfma(f, ones, zero);
        }
        const f32x4 cv = tok == 0 ? c4[0] : (tok == 1 ? c4[1] : (tok == 2 ? c4[2] : c4[3]));
        if (tok < 4 && !isW2) __builtin_amdgcn_raw_buffer_store_b128(__builtin_bit_cast(v4u, cv), rs, tok * 64 + g4 * 16, WS_CS1 + (uks * 1792 + ulg * 64) * 4, AUX_SC1);
      }
    }
    VMWAIT(0);
    setp(L, wave, lane, 12);
    STAMP(1);
  }
  STAMP(2);
  const int hh = wave & 1, qg = wave >> 1;
  u64 fl[5];
  if (wave == 0) {
    {
      waitp(L, 0, 13);
      if (tj != 15) waitp(L, 1, 12);
      if (f1act || lane == 12) __hip_atomic_store(f1dst, tag, RLX_AGENT);
    }
    STAMP(15);
  }
  int go[8], lo[8];
  {
    const int isV = wave >> 2, t0 = 6 * (wave & 3);
    const int tmin = 16 - 8 * gi - t0;
#pragma unroll
    for (int e = 0; e < 8; ++e) {
      const int x = e * 64 + lane, t4 = x / 80, rem = x - t4 * 80;
      const bool ok = x < 480 && t4 >= tmin;
      lo[e] = ok ? (isV ? L_V + (t0 + t4) * V_TS + (rem >> 4) * 320 + tok * 16 : L_K + t0 * 1280 + x * 16) : L_CS + (lane & 15) * 16;
      go[e] = ok ? t4 * 20480 + rem * 16 : 0x7FFFFFF0;
    }
  }
  const int qo = ((8 * gi + 2 * qg + ((lane & 31) >> 4)) * 80 + 5 * gj) * 256 + tok * 16;
  const int r32 = lane & 31, h = lane >> 5, dg = (lane >> 4) & 1;
  const float NEG = -__builtin_inff();
  LAS char* k1p = L + L_K + (2 * qg + (r32 >> 4)) * 1280 + (r32 & 15) * 16;
  LAS char* k0p = k1p + (3 * hh + h) * 256;
  k1p += 512;
  LAS char* vp[2];
#pragma unroll
  for (int t = 0; t < 2; ++t) {
    const int key = 8 * t + 4 * h + q4, gam = 5 * hh + (dg ? 4 : p4);
    const int off = (dg && p4) ? 1600 : (gam >> 1) * 320 + key * 16 + (gam & 1) * 8;
    vp[t] = L + L_V + (2 * qg) * V_TS + off;
  }
  k0p = opaque(k0p); k1p = opaque(k1p); vp[0] = opaque(vp[0]); vp[1] = opaque(vp[1]);
  if (wave == 0) {
    const int slot = lane >> 1;
    const int row = slot == 0 ? gi : gi - 2 + (slot < 4 ? slot - 1 : slot - 4);
    const bool act = lane < 14 && row >= 0 && ((lane & 1) == 0 || ((10 * gj) >> 5) != ((10 * gj + 9) >> 5));
    poll_flags(inbox + me * 16 + (lane & 15), act, tag);
    waitp1(L, 4, 13);
    setp(L, wave, lane, 14);
  } else if (!isM) {
    if (wave == 4) {
      GET_TAG()
      poll_flags(flags + (4 * 16 + 0) * 16 + tok, lane < 16, tag);
      setp(L, wave, lane, 13);
    } else waitp1(L, 4, 13);
    waitp(L, 0, 13);
    const int wrot = 22 * (2 * (gi & 1) + rh);
#pragma unroll 1
    for (int e = v; e < 90; e += 4) { int u = e + wrot; u = u >= 90 ? u - 90 : u; const int ks = u / 5;
      dmas(rsi, l16, WS_WOT + ct * 5120 + KS3(ks) * 40960 + (u - 5 * ks) * 1024, lds0 + L_WO + u * 1024); }
    waitp1(L, 0, 14);
  } else waitp1(L, 0, 14);
  STAMP(3);
  v4u q0, q1;
  {
    const int gb = WS_IMG + (((1 + (wave >> 2)) * 128 + 8 * gi - 16 + 6 * (wave & 3)) * 80 + 5 * gj) * 256;
    v4u hv[8];
#pragma unroll
    for (int e = 0; e < 8; ++e) hv[e] = __builtin_amdgcn_raw_buffer_load_b128(rs, go[e], gb, AUX_SC1);
    q0 = __builtin_amdgcn_raw_buffer_load_b128(rs, qo + (3 * hh + (lane >> 5)) * 256, WS_IMG, AUX_SC1);
    q1 = __builtin_amdgcn_raw_buffer_load_b128(rs, qo + 512, WS_IMG, AUX_SC1);
    if (wave == 7) {
#pragma unroll
      for (int r = 0; r < 4; ++r) fl[r] = __hip_atomic_load(flags + r * 64 + lane, RLX_AGENT);
      fl[4] = __hip_atomic_load(flags + (4 * 16 + 1) * 16 + (lane & 15), RLX_AGENT);
    }
    SCHED();
    STAMP(16);
    if (wave == 2 && lane < 24) *(LAS v4u*)(L + L_V + lane * V_TS + 1600) = ones;
#pragma unroll
    for (int e = 0; e < 8; ++e) *(LAS v4u*)(L + lo[e]) = hv[e];
    GET_TAG()
    if (isM) {
      float km = 0.f;
#pragma unroll
      for (int e = 0; e < 8; ++e) km = fmaxf(km, dot2(hv[e].x, hv[e].x, dot2(hv[e].y, hv[e].y, dot2(hv[e].z, hv[e].z, dot2(hv[e].w, hv[e].w, 0.f)))));
      km = km == km ? km : __builtin_inff();
      km = fmaxf(km, __int_as_float(__builtin_amdgcn_update_dpp(0, __float_as_int(km), 0x128, 0xf, 0xf, false)));
      km = fmaxf(km, __int_as_float(__builtin_amdgcn_update_dpp(0, __float_as_int(km), 0x124, 0xf, 0xf, false)));
      km = fmaxf(km, __int_as_float(__builtin_amdgcn_update_dpp(0, __float_as_int(km), 0x122, 0xf, 0xf, false)));
      km = fmaxf(km, __int_as_float(__builtin_amdgcn_update_dpp(0, __float_as_int(km), 0x121, 0xf, 0xf, false)));
      km = max4(km);
      if (lane == 0) *(LAS float*)(L + L_PROG + 32 + wave * 4) = km;
    }
    if (wave == 7) {
      const bool ok = __all(fl[0] == tag && fl[1] == tag && fl[2] == tag && fl[3] == tag && fl[4] == tag);
      if (lane == 0) *(LAS u32*)(L + L_PROG + 48) = ok ? 1u : 0u;
    }
  }
  if (lane >= 32) q1 = v4u{0u, 0u, 0u, 0u};
  else if (hh == 0) { q1.z = 0u; q1.w = 0u; }
  else { q1.x = 0u; q1.y = 0u; }
  BAR();
  STAMP(4);
  {
    f32x16 o, z16;
#pragma unroll
    for (int i = 0; i < 16; ++i) { o[i] = 0.f; z16[i] = 0.f; }
    const int dq = r32 - 4 * h;
#define UP(i) (((i) & 3) + 8 * ((i) >> 2) > dq)
#define QK(b, c) mfma32(*(LAS v4u*)(k1p + (b) * 2560), q1, mfma32(*(LAS v4u*)(k0p + (b) * 2560), q0, c))
#define MASK_EDGE(s) _Pragma("unroll") for (int i = 0; i < 16; ++i) s[i] = UP(i) ? s[i] : NEG
#define MASK_DIAG(s) _Pragma("unroll") for (int i = 0; i < 16; ++i) s[i] = UP(i) ? NEG : s[i]
#define MAXS(s) _Pragma("unroll") for (int i = 0; i < 16; i += 4) { m = max3(m, s[i], s[i + 1]); m2 = max3(m2, s[i + 2], s[i + 3]); }
#define EX(x) __builtin_amdgcn_exp2f(x)
#define PV(b, s) { const v4u pa = {pk2(EX(s[0]), EX(s[1])), pk2(EX(s[2]), EX(s[3])), pk2(EX(s[4]), EX(s[5])), pk2(EX(s[6]), EX(s[7]))}; \
      const v4u pb = {pk2(EX(s[8]), EX(s[9])), pk2(EX(s[10]), EX(s[11])), pk2(EX(s[12]), EX(s[13])), pk2(EX(s[14]), EX(s[15]))}; \
      o = mfma32(trfrag(vp[0] + (b) * (2 * V_TS), vp[1] + (b) * (2 * V_TS)), pa, o); o = mfma32(trfrag(vp[0] + (b) * (2 * V_TS) + V_TS, vp[1] + (b) * (2 * V_TS) + V_TS), pb, o); }
    if (!isM) __builtin_amdgcn_s_setprio(1);
    const bool flok = __builtin_amdgcn_readfirstlane(*(LAS u32*)(L + L_PROG + 48)) != 0u;
    if (!flok) {
      poll_flags(flags + (4 * 16 + 1) * 16 + (lane & 15), true, tag);
#pragma unroll 1
      for (int r = 0; r < 4; ++r) poll_flags(flags + r * 64 + lane, true, tag);
    }
    if (bid == 0 && tid == 0) __hip_atomic_store((GAS u64*)(P.ws + WS_EPOCH), tag, RLX_AGENT);
    const int bmin = 8 - 4 * gi - qg;
    float m;
    {
      const f32x4 km4 = *(LAS f32x4*)(L + L_PROG + 32);
      float qn = dot2(q0.x, q0.x, dot2(q0.y, q0.y, dot2(q0.z, q0.z, dot2(q0.w, q0.w, dot2(q1.x, q1.x, dot2(q1.y, q1.y, dot2(q1.z, q1.z, dot2(q1.w, q1.w, 0.f))))))));
      auto r = __builtin_amdgcn_permlane32_swap(__float_as_uint(qn), __float_as_uint(qn), false, false);
      qn = __uint_as_float(r[0]) + __uint_as_float(r[1]);
      m = 1.01f * sqrtf(qn * 3.f * max3(fmaxf(km4[0], km4[1]), km4[2], km4[3])) + 0.01f;
    }
    if (!__all(m <= 40.f)) {
      float m2 = NEG;
      m = NEG;
      { f32x16 s = QK(8, z16); MASK_DIAG(s); MAXS(s) }
      if (bmin <= 0) { f32x16 s = QK(0, z16); MASK_EDGE(s); MAXS(s) }
#pragma unroll 1
      for (int b = bmin > 1 ? bmin : 1; b < 8; ++b) { f32x16 s = QK(b, z16); MAXS(s) }
      m = max3(m, m2, m2);
      auto r = __builtin_amdgcn_permlane32_swap(__float_as_uint(m), __float_as_uint(m), false, false);
      m = max3(m, __uint_as_float(r[0]), __uint_as_float(r[1]));
    }
    f32x16 nm;
#pragma unroll
    for (int i = 0; i < 16; ++i) nm[i] = -m;
    if (bmin <= 0) {
      f32x16 s = QK(0, nm);
#pragma unroll
      for (int b = 0; b < 9; ++b) {
        f32x16 sn = s;
        if (b < 8) sn = QK(b + 1, nm);
        if (b == 0) MASK_EDGE(s);
        if (b == 8) MASK_DIAG(s);
        PV(b, s)
        s = sn;
      }
    } else {
      { f32x16 s = QK(8, nm); MASK_DIAG(s); PV(8, s) }
#pragma unroll 1
      for (int b = bmin; b < 8; ++b) { f32x16 s = QK(b, nm); PV(b, s) }
    }
    const float inv = __builtin_amdgcn_rcpf(o[12]);
    const int mt = 2 * qg + (r32 >> 4);
    const int ob = WS_OF + (gi * 8) * 20 * 1024;
    __builtin_amdgcn_raw_buffer_store_b128(
        v4u{pk2(o[0] * inv, o[1] * inv), pk2(o[2] * inv, o[3] * inv), pk2(o[4] * inv, o[5] * inv), pk2(o[6] * inv, o[7] * inv)}, rs,
        mt * 20480 + ((2 * hh + h) * 16 + tok) * 16, ob + gj * 1024, AUX_SC1);
    if (h == 0)
      __builtin_amdgcn_raw_buffer_store_b64(v2u{pk2(o[8] * inv, o[9] * inv), pk2(o[10] * inv, o[11] * inv)}, rs,
          mt * 20480 + tok * 16, ob + (16 + (gj >> 2)) * 1024 + (gj & 3) * 256 + 8 * hh, AUX_SC1);
  }
  __builtin_amdgcn_s_setprio(0);
  VMWAIT(0);
  BAR();
  STAMP(5);
  const int mt3 = 4 * rh + v;
  if (isM) {
    u32 hres[10];
    if (wave == 0 && lane < 16) __hip_atomic_store(inbox + (1 * 256 + gi * 16 + lane) * 16 + gj, tag, RLX_AGENT);
    v2u xres[5];
    const GAS char* xr = (const GAS char*)P.x + (size_t)(128 * gi + 16 * mt3 + tok) * 1280 + ct * 160 + g4 * 8;
#pragma unroll
    for (int t = 0; t < 5; ++t) xres[t] = *(const GAS v2u*)(xr + t * 32);
    if (wave == 0) { poll_flags(inbox + (1 * 256 + me) * 16 + (lane & 15), lane < 16, tag); setp(L, wave, lane, 17); }
    else waitp1(L, 0, 17);
    STAMP(6);
    {
      v4u a[20];
      const int ab = WS_OF + (gi * 8 + mt3) * 20 * 1024;
#pragma unroll
      for (int s = 0; s < 20; ++s) { const int ks = KS3(s); a[s] = __builtin_amdgcn_raw_buffer_load_b128(rs, l16, ab + ks * 1024, AUX_SC1); }
      SCHED();
      setp(L, wave, lane, 18);
      STAMP(17);
      f32x4 acc[5];
#pragma unroll
      for (int n = 0; n < 5; ++n) acc[n] = zero;
      LAS char* bb = L + L_WO + l16;
      LAS char* bh = opaque(bb + 12 * 5120);
      v4u bf[3][5];
#pragma unroll
      for (int k = 0; k < 2; ++k) {
        LAS char* b = bb + k * 5120;
#pragma unroll
        for (int n = 0; n < 5; ++n) bf[k][n] = *(LAS v4u*)(b + n * 1024);
      }
#pragma unroll
      for (int s = 0; s < 20; ++s) {
        if (s == 16) waitp(L, 1, 14);
        if (s + 2 < 20) {
          LAS char* b = s + 2 < 12 ? bb + (s + 2) * 5120 : bh + (s + 2 - 12) * 5120;
#pragma unroll
          for (int n = 0; n < 5; ++n) bf[(s + 2) % 3][n] = *(LAS v4u*)(b + n * 1024);
        }
#pragma unroll
        for (int n = 0; n < 5; ++n) acc[n] = mfma(bf[s % 3][n], a[s], acc[n]);
#pragma unroll
        for (int n = 0; n < 5; ++n) { SGB(0x8, 1); SGB(0x100, 1); }
        SCHED();
      }
      setp(L, wave, lane, 19);
      STAMP(18);
      float s1 = 0.f, s2 = 0.f;
#pragma unroll
      for (int n = 0; n < 5; ++n) {
        const float h0 = bflo(xres[n].x) + rbf(acc[n][0]), h1 = bfhi(xres[n].x) + rbf(acc[n][1]);
        const float h2 = bflo(xres[n].y) + rbf(acc[n][2]), h3 = bfhi(xres[n].y) + rbf(acc[n][3]);
        hres[2 * n] = pk2(h0, h1); hres[2 * n + 1] = pk2(h2, h3);
        s1 = dot2(hres[2 * n], 0x3F803F80u, s1); s1 = dot2(hres[2 * n + 1], 0x3F803F80u, s1);
        s2 = dot2(hres[2 * n], hres[2 * n], s2); s2 = dot2(hres[2 * n + 1], hres[2 * n + 1], s2);
      }
      const int hb = WS_HF + (gi * 8 + mt3) * 20 * 1024 + ((5 * ct) >> 1) * 1024;
      if ((ct & 1) == 0) {
        __builtin_amdgcn_raw_buffer_store_b128(v4u{hres[0], hres[1], hres[2], hres[3]}, rs, l16, hb, AUX_SC1);
        __builtin_amdgcn_raw_buffer_store_b128(v4u{hres[4], hres[5], hres[6], hres[7]}, rs, l16, hb + 1024, AUX_SC1);
        __builtin_amdgcn_raw_buffer_store_b64(v2u{hres[8], hres[9]}, rs, l16, hb + 2048, AUX_SC1);
      } else {
        __builtin_amdgcn_raw_buffer_store_b64(v2u{hres[0], hres[1]}, rs, l16 + 8, hb, AUX_SC1);
        __builtin_amdgcn_raw_buffer_store_b128(v4u{hres[2], hres[3], hres[4], hres[5]}, rs, l16, hb + 1024, AUX_SC1);
        __builtin_amdgcn_raw_buffer_store_b128(v4u{hres[6], hres[7], hres[8], hres[9]}, rs, l16, hb + 2048, AUX_SC1);
      }
      s1 = sum4(s1); s2 = sum4(s2);
      if (g4 == 0)
        __builtin_amdgcn_raw_buffer_store_b64(v2u{__float_as_uint(s1), __float_as_uint(s2)}, rs,
            WS_ST + (128 * gi + 16 * mt3 + tok) * 64 + ct * 8, 0, AUX_SC1);
      setp(L, wave, lane, 20);
      VMWAIT(0);
      setp(L, wave, lane, 21);
    }
    STAMP(7);
    if (wave == 0) {
      waitp(L, 0, 21);
      if (lane < 16) __hip_atomic_store(inbox + (2 * 256 + gi * 16 + lane) * 16 + gj, tag, RLX_AGENT);
      poll_flags(inbox + (2 * 256 + me) * 16 + (lane & 15), lane < 16, tag);
      setp(L, wave, lane, 22);
    } else waitp1(L, 0, 22);
    STAMP(8);
    {
      constexpr int W = 10, W0 = 4;
      v4u a[W][2];
      const int ab = WS_HF + (gi * 8 + 2 * wave) * 20 * 1024;
#define A4LD(s) { a[(s) % W][0] = __builtin_amdgcn_raw_buffer_load_b128(rs, l16, ab + KS4(s) * 1024, AUX_SC1); \
        a[(s) % W][1] = __builtin_amdgcn_raw_buffer_load_b128(rs, l16, ab + (20 + KS4(s)) * 1024, AUX_SC1); }
#pragma unroll
      for (int s = 0; s < W0; ++s) A4LD(s)
      v4u stv[2];
#pragma unroll
      for (int m = 0; m < 2; ++m)
        stv[m] = __builtin_amdgcn_raw_buffer_load_b128(rs, WS_ST + (128 * gi + 32 * wave + 16 * m + tok) * 64 + g4 * 16, 0, AUX_SC1);
      SCHED();
      f32x4 acc[2][7];
#pragma unroll
      for (int m = 0; m < 2; ++m)
#pragma unroll
        for (int n = 0; n < 7; ++n) acc[m][n] = zero;
      LAS char* bb = L + l16;
      LAS char* bA = opaque(bb + L_W1A);
      LAS char* bC = opaque(bb + 8 * 7168);
#define W1P(p) ((p) < 8 ? bA + (p) * 7168 : ((p) < 16 ? bb + ((p) - 8) * 7168 : bC + ((p) - 16) * 7168))
      waitp(L, 1, 15);
      STAMP(19);
      v4u bf[2][7];
      {
        LAS char* b = W1P(0);
#pragma unroll
        for (int n = 0; n < 7; ++n) bf[0][n] = *(LAS v4u*)(b + n * 1024);
      }
#pragma unroll
      for (int s = 0; s < 20; ++s) {
        if (s + 1 < 20) {
          LAS char* b = W1P(s + 1);
#pragma unroll
          for (int n = 0; n < 7; ++n) bf[(s + 1) & 1][n] = *(LAS v4u*)(b + n * 1024);
        }
        const v4u a0 = a[s % W][0], a1 = a[s % W][1];
#pragma unroll
        for (int j = RAMP(s, W0, W); j < RAMP(s + 1, W0, W); ++j) if (j < 20) A4LD(j)
#pragma unroll
        for (int n = 0; n < 7; ++n) {
          acc[0][n] = mfma(bf[s & 1][n], a0, acc[0][n]);
          acc[1][n] = mfma(bf[s & 1][n], a1, acc[1][n]);
        }
#pragma unroll
        for (int n = 0; n < 7; ++n) { SGB(0x8, 2); SGB(0x100, 1); }
        SCHED();
        if (s == 10 && lane == 0) *(volatile LAS u32*)(L + L_PROG + wave * 4) = 23u;
      }
      setp(L, wave, lane, 24);
      STAMP(12);
      float mb2[2], rs2[2];
#pragma unroll
      for (int m = 0; m < 2; ++m) {
        const float t1 = sum4(__uint_as_float(stv[m].x) + __uint_as_float(stv[m].z));
        const float t2 = sum4(__uint_as_float(stv[m].y) + __uint_as_float(stv[m].w));
        const float mean = t1 * (1.f / 640.f);
        const float var = fmaxf(t2 * (1.f / 640.f) - mean * mean, 0.f);
        mb2[m] = rbf(mean);
        rs2[m] = 1.f / rbf(sqrtf(rbf(rbf(var) + 1e-5f)));
      }
      u32 hp[2][7][2];
#pragma unroll
      for (int n = 0; n < 7; ++n) {
        const f32x4 cs = *(LAS f32x4*)(L + L_CS + (16 * n + 4 * g4) * 4);
#pragma unroll
        for (int m = 0; m < 2; ++m) {
          hp[m][n][0] = pk2(fmaxf((acc[m][n][0] - mb2[m] * cs[0]) * rs2[m], 0.f), fmaxf((acc[m][n][1] - mb2[m] * cs[1]) * rs2[m], 0.f));
          hp[m][n][1] = pk2(fmaxf((acc[m][n][2] - mb2[m] * cs[2]) * rs2[m], 0.f), fmaxf((acc[m][n][3] - mb2[m] * cs[3]) * rs2[m], 0.f));
        }
      }
      const int fbs = WS_FF + ((gi * 8 + 2 * wave) * 54 + (T0 >> 1)) * 1024;
#pragma unroll
      for (int m = 0; m < 2; ++m) {
        const int fb = fbs + m * 54 * 1024;
        if ((T0 & 1) == 0) {
#pragma unroll
          for (int e = 0; e < 3; ++e)
            __builtin_amdgcn_raw_buffer_store_b128(v4u{hp[m][2 * e][0], hp[m][2 * e][1], hp[m][2 * e + 1][0], hp[m][2 * e + 1][1]}, rs, l16, fb + e * 1024, AUX_SC1);
          if (nT == 7) __builtin_amdgcn_raw_buffer_store_b64(v2u{hp[m][6][0], hp[m][6][1]}, rs, l16, fb + 3 * 1024, AUX_SC1);
        } else {
          __builtin_amdgcn_raw_buffer_store_b64(v2u{hp[m][0][0], hp[m][0][1]}, rs, l16 + 8, fb, AUX_SC1);
#pragma unroll
          for (int e = 0; e < 3; ++e)
            __builtin_amdgcn_raw_buffer_store_b128(v4u{hp[m][2 * e + 1][0], hp[m][2 * e + 1][1], hp[m][2 * e + 2][0], hp[m][2 * e + 2][1]}, rs, l16, fb + (e + 1) * 1024, AUX_SC1);
        }
      }
      VMWAIT(0);
      setp(L, wave, lane, 25);
    }
    STAMP(9);
    if (wave == 0) {
      waitp(L, 0, 25);
      if (lane < 16) __hip_atomic_store(inbox + (3 * 256 + gi * 16 + lane) * 16 + gj, tag, RLX_AGENT);
      poll_flags(inbox + (3 * 256 + me) * 16 + (lane & 15), lane < 16, tag);
      setp(L, wave, lane, 26);
    } else waitp1(L, 0, 26);
    STAMP(10);
    {
      const int mh = wave >> 1, kp = wave & 1;
      constexpr int W = 12, W0 = 4;
      v4u a[W][2];
      const int ab = WS_FF + (gi * 8 + 4 * rh + 2 * mh) * 54 * 1024;
#define A5LD(i) { const int o_ = ab + KS5(2 * (i) + kp) * 1024; a[(i) % W][0] = __builtin_amdgcn_raw_buffer_load_b128(rs, l16, o_, AUX_SC1); \
        a[(i) % W][1] = __builtin_amdgcn_raw_buffer_load_b128(rs, l16, o_ + 54 * 1024, AUX_SC1); }
#pragma unroll
      for (int i = 0; i < W0; ++i) A5LD(i)
      SCHED();
      f32x4 acc[2][5];
#pragma unroll
      for (int m = 0; m < 2; ++m)
#pragma unroll
        for (int n = 0; n < 5; ++n) acc[m][n] = zero;
      LAS char* bb = L + L_W2 + kp * 5120 + l16;
      LAS char* b1 = opaque(bb + 12 * 5120);
      LAS char* b2 = opaque(bb + 24 * 5120);
#define W2P(i) ((i) % 15 < 6 ? bb + ((i) % 15) * 10240 : ((i) % 15 < 12 ? b1 + ((i) % 15 - 6) * 10240 : b2 + ((i) % 15 - 12) * 10240))
      waitp(L, 1, 18);
      STAMP(20);
      v4u bf[2][5];
#pragma unroll
      for (int n = 0; n < 5; ++n) bf[0][n] = *(LAS v4u*)(W2P(0) + n * 1024);
#pragma unroll
      for (int i = 0; i < 27; ++i) {
        if (i == 14) waitp(L, 1, 19);
        if (i == 20) waitp(L, 1, 20);
        if (i + 1 < 27) {
#pragma unroll
          for (int n = 0; n < 5; ++n) bf[(i + 1) & 1][n] = *(LAS v4u*)(W2P(i + 1) + n * 1024);
        }
        const v4u a0 = a[i % W][0], a1 = a[i % W][1];
#pragma unroll
        for (int j = RAMP(i, W0, W); j < RAMP(i + 1, W0, W); ++j) if (j < 27) A5LD(j)
#pragma unroll
        for (int n = 0; n < 5; ++n) {
          acc[0][n] = mfma(bf[i & 1][n], a0, acc[0][n]);
          acc[1][n] = mfma(bf[i & 1][n], a1, acc[1][n]);
        }
#pragma unroll
        for (int n = 0; n < 5; ++n) { SGB(0x8, 2); SGB(0x100, 1); }
        SCHED();
        if (i == 5) setp(L, wave, lane, 27);
        if (i == 11) setp(L, wave, lane, 28);
        if (i == 15) setp(L, wave, lane, 29);
      }
      LAS char* xs = L + L_W2 + 24 * 5120 + l16;
      waitp(L, 0, 29);
#pragma unroll
      for (int n = 0; n < 5; ++n) *(LAS f32x4*)(xs + (wave ^ 1) * 5120 + n * 1024) = kp ? acc[0][n] : acc[1][n];
      setp(L, wave, lane, 30);
      waitp1(L, wave ^ 1, 30);
      GAS char* orow = (GAS char*)P.out + (size_t)(128 * gi + 16 * mt3 + tok) * 1280 + ct * 160 + g4 * 8;
#pragma unroll
      for (int n = 0; n < 5; ++n) {
        const f32x4 t = (kp ? acc[1][n] : acc[0][n]) + *(LAS f32x4*)(xs + wave * 5120 + n * 1024);
        v2u o;
        o.x = pk2(bflo(hres[2 * n]) + rbf(t[0]), bfhi(hres[2 * n]) + rbf(t[1]));
        o.y = pk2(bflo(hres[2 * n + 1]) + rbf(t[2]), bfhi(hres[2 * n + 1]) + rbf(t[3]));
        *(GAS v2u*)(orow + n * 32) = o;
      }
    }
  } else {
    {
#pragma unroll
      for (int e = 0; e < 3; ++e) { int u = 90 + v + 4 * e; u = u > 99 ? 99 : u; const int ks = u / 5; dmas(rsi, l16, WS_WOT + ct * 5120 + ks * 40960 + (u - 5 * ks) * 1024, lds0 + L_WO + u * 1024); }
    }
#pragma unroll
    for (int e = 0; e < 14; ++e) { int u = v + 4 * e + 28 * (gi & 1); u = u >= 56 ? u - 56 : u; const int ks = u / 7; dmas(rsi, l16, WS_W1T + (KS4(ks) * 108 + T0 + (u - 7 * ks)) * 1024, lds0 + L_W1A + u * 1024); }
    VMWAIT(14);
    setp(L, wave, lane, 14);
    if (wave == 7) {
      v4u cp[20];
#pragma unroll
      for (int k = 0; k < 20; ++k) cp[k] = __builtin_amdgcn_raw_buffer_load_b128(rs, (lane < 28 ? lane : 0) * 16, WS_CS1 + (k * 1792 + 16 * T0) * 4, AUX_SC1);
      f32x4 c4 = zero;
#pragma unroll
      for (int k = 0; k < 20; ++k) c4 += __builtin_bit_cast(f32x4, cp[k]);
      if (lane < 28) *(LAS f32x4*)(L + L_CS + lane * 16) = c4;
    }
    STAMP(6);
#define W2LD(u) __builtin_amdgcn_raw_buffer_load_b128(rs, l16, WS_W2T + (KS5((u) / 5) * 40 + 5 * ct + ((u) - 5 * ((u) / 5))) * 1024, AUX_SC1)
    const int w2rot = 2 * (gi & 1) + rh;
    waitps(L, 0, 18);
    {
      v4u pre[21];
#pragma unroll
      for (int e = 0; e < 21; ++e) { int u = v + 4 * e + 42 * (gi & 1); u = u >= 84 ? u - 84 : u; const int ks = u / 7;
        pre[e] = __builtin_amdgcn_raw_buffer_load_b128(rs, l16, WS_W1T + (KS4(8 + ks) * 108 + T0 + (u - 7 * ks)) * 1024, AUX_SC1); }
      SCHED();
      waitp(L, 0, 19);
#pragma unroll
      for (int e = 0; e < 21; ++e) { int u = v + 4 * e + 42 * (gi & 1); u = u >= 84 ? u - 84 : u; *(LAS v4u*)(L + L_W1B + u * 1024 + l16) = pre[e]; }
    }
    setp(L, wave, lane, 15);
    STAMP(7);
    v4u wb[20], wc[25];
#define ROT(x, n, r) ((x) + (r) >= (n) ? (x) + (r) - (n) : (x) + (r))
    waitp(L, 0, 21);
#pragma unroll
    for (int e = 0; e < 20; ++e) { const int u = ROT(v + 4 * e, 80, 20 * w2rot); wb[e] = W2LD(u); }
    SCHED();
    STAMP(8);
    waitps(L, 0, 23);
#pragma unroll
    for (int e = 0; e < 25; ++e) { const int u = 80 + ROT(v + 4 * e, 100, 0); wc[e] = W2LD(u); }
    SCHED();
    waitp(L, 0, 24);
#pragma unroll
    for (int e = 0; e < 20; ++e) { const int u = ROT(v + 4 * e, 80, 20 * w2rot); *(LAS v4u*)(L + L_W2 + u * 1024 + l16) = wb[e]; }
#pragma unroll
    for (int e = 0; e < 18; ++e) { const int u = 80 + v + 4 * e; if (u < 150) *(LAS v4u*)(L + L_W2 + u * 1024 + l16) = wc[e]; }
    setp(L, wave, lane, 18);
    STAMP(10);
    v4u wd[23];
    waitp(L, 0, 25);
#pragma unroll
    for (int e = 0; e < 23; ++e) { int u = 180 + v + 4 * e; u = u > 269 ? 269 : u; wd[e] = W2LD(u); }
    SCHED();
#define W2PUT(u, val) { *(LAS v4u*)(L + L_W2 + ((u) - 150) * 1024 + l16) = (val); }
    waitp(L, 0, 27);
#pragma unroll
    for (int e = 17; e < 25; ++e) { const int u = 80 + v + 4 * e; if (u >= 150) W2PUT(u, wc[e]) }
#pragma unroll
    for (int e = 0; e < 8; ++e) { const int u = 180 + v + 4 * e; if (u < 210) W2PUT(u, wd[e]) }
    setp(L, wave, lane, 19);
    waitp(L, 0, 28);
#pragma unroll
    for (int e = 7; e < 23; ++e) { const int u = 180 + v + 4 * e; if (u >= 210 && u < 270) W2PUT(u, wd[e]) }
    setp(L, wave, lane, 20);
  }
  STAMP(11);
  asm volatile("fb_code_end:");
}
}

extern "C" void kernel_launch(void* const* d_in, const int* in_sizes, int n_in, void* d_out, int out_size, void* d_ws,
                              size_t ws_size, hipStream_t stream) {
  fb::Params P;
  P.x = (const char*)d_in[0]; P.wqkv = (const char*)d_in[1]; P.wo = (const char*)d_in[2];
  P.w1 = (const char*)d_in[4]; P.w2 = (const char*)d_in[5]; P.out = (char*)d_out; P.ws = (char*)d_ws;
  (void)hipFuncSetAttribute((const void*)fb::fused_block, hipFuncAttributeMaxDynamicSharedMemorySize, fb::LDS_BYTES);
  fb::fused_block<<<256, 512, fb::LDS_BYTES, stream>>>(P);
}
```

```cpp
#include <hip/hip_runtime.h>
#include <hip/hip_bf16.h>
#include <stdint.h>

typedef unsigned short u16;
typedef unsigned int u32;
typedef unsigned long long u64;
typedef u32 v4u __attribute__((ext_vector_type(4)));
typedef u32 v2u __attribute__((ext_vector_type(2)));
typedef float f32x4 __attribute__((ext_vector_type(4)));
typedef float f32x2 __attribute__((ext_vector_type(2)));
typedef float f32x16 __attribute__((ext_vector_type(16)));
typedef short s16x4 __attribute__((ext_vector_type(4)));
typedef __bf16 bf16x8 __attribute__((ext_vector_type(8)));
typedef __bf16 bf16x2 __attribute__((ext_vector_type(2)));
#define LAS __attribute__((address_space(3)))
#define GAS __attribute__((address_space(1)))
#define DI __device__ __forceinline__
#define RLX_AGENT __ATOMIC_RELAXED, __HIP_MEMORY_SCOPE_AGENT

#define STAMPS 0
#define CODE_PF 1

namespace fb {
constexpr int WS_EPOCH = 0;
constexpr int WS_FLAGS = 4096;
constexpr int WS_INBOX = 16384;
constexpr int WS_IMG = WS_INBOX + 4 * 256 * 128;
constexpr int WS_OF = WS_IMG + 3 * 128 * 20480;
constexpr int WS_HF = WS_OF + 16 * 8 * 20 * 1024;
constexpr int WS_ST = WS_HF + 16 * 8 * 20 * 1024;
constexpr int WS_FF = WS_ST + 2048 * 64;
constexpr int WS_WOT = WS_FF + 16 * 8 * 54 * 1024;
constexpr int WS_W1T = WS_WOT + 20 * 40 * 1024;
constexpr int WS_W2T = WS_W1T + 20 * 108 * 1024;
constexpr int WS_CS1 = WS_W2T + 54 * 40 * 1024;
constexpr int WS_ST0 = WS_CS1 + 20 * 1792 * 4;
constexpr int WS_END = WS_ST0 + 2048 * 8;
constexpr int WS_STAMP = WS_END;
constexpr int LDS_BYTES = 163840;
constexpr int L_RING = 0;
constexpr int L_XR = 81920;
constexpr int L_UNIT = 147456;
constexpr int L_ST1 = 159744, L_EP = 161792;
constexpr int L_TAB = 162816;
constexpr int L_CS = L_TAB, L_PROG = L_TAB + 512;
constexpr int L_WO = 0;
constexpr int L_K = 92160;
constexpr int L_V = L_K + 24 * 1280, V_TS = 1616;
constexpr int L_W1A = 102400;
constexpr int L_W1B = 0;
constexpr int L_W2 = 0;
constexpr int AUX_SC1 = 16;
constexpr u32 SPIN_CAP = 1u << 17;

DI f32x4 mfma(v4u a, v4u b, f32x4 c) {
  return __builtin_amdgcn_mfma_f32_16x16x32_bf16(__builtin_bit_cast(bf16x8, a), __builtin_bit_cast(bf16x8, b), c, 0, 0, 0);
}
DI f32x16 mfma32(v4u a, v4u b, f32x16 c) {
  return __builtin_amdgcn_mfma_f32_32x32x16_bf16(__builtin_bit_cast(bf16x8, a), __builtin_bit_cast(bf16x8, b), c, 0, 0, 0);
}
DI float max3(float a, float b, float c) { return __builtin_fmaxf(__builtin_fmaxf(a, b), c); }
DI v2u trr(LAS char* p) { return __builtin_bit_cast(v2u, __builtin_amdgcn_ds_read_tr16_b64_v4i16((LAS s16x4*)p)); }
DI v4u trfrag(LAS char* p0, LAS char* p1) { v2u a = trr(p0), b = trr(p1); return v4u{a.x, a.y, b.x, b.y}; }
DI u32 pk2(float a, float b) { f32x2 v = {a, b}; return __builtin_bit_cast(u32, __builtin_convertvector(v, bf16x2)); }
DI float bflo(u32 u) { return __uint_as_float(u << 16); }
DI float bfhi(u32 u) { return __uint_as_float(u & 0xFFFF0000u); }
DI float rbf(float f) { return bflo(pk2(f, 0.f)); }
DI float dot2(u32 a, u32 b, float c) {
  return __builtin_amdgcn_fdot2_f32_bf16(__builtin_bit_cast(bf16x2, a), __builtin_bit_cast(bf16x2, b), c, false);
}
DI float sum4(float v) {
  auto r = __builtin_amdgcn_permlane16_swap(__float_as_uint(v), __float_as_uint(v), false, false);
  v = __uint_as_float(r[0]) + __uint_as_float(r[1]);
  auto s = __builtin_amdgcn_permlane32_swap(__float_as_uint(v), __float_as_uint(v), false, false);
  return __uint_as_float(s[0]) + __uint_as_float(s[1]);
}
DI float max4(float v) {
  auto r = __builtin_amdgcn_permlane16_swap(__float_as_uint(v), __float_as_uint(v), false, false);
  v = fmaxf(__uint_as_float(r[0]), __uint_as_float(r[1]));
  auto s = __builtin_amdgcn_permlane32_swap(__float_as_uint(v), __float_as_uint(v), false, false);
  return fmaxf(__uint_as_float(s[0]), __uint_as_float(s[1]));
}
typedef int v4i __attribute__((ext_vector_type(4)));
DI v4i mkrsrc(const void* p, int bytes) { return v4i{(int)(u32)(uintptr_t)p, (int)((uintptr_t)p >> 32), bytes, 0x00020000}; }
DI void dmab(v4i rsrc, u32 voff, u32 soff, u32 lds_dst) {
  asm volatile("s_mov_b32 m0, %3\n\ts_nop 0\n\tbuffer_load_dwordx4 %0, %1, %2 offen lds"
               :: "v"(voff), "s"(rsrc), "s"(__builtin_amdgcn_readfirstlane(soff)), "s"(__builtin_amdgcn_readfirstlane(lds_dst)) : "memory", "m0");
}
DI void dmas(v4i rsrc, u32 voff, u32 soff, u32 lds_dst) {
  asm volatile("s_mov_b32 m0, %3\n\ts_nop 0\n\tbuffer_load_dwordx4 %0, %1, %2 offen sc1 lds"
               :: "v"(voff), "s"(rsrc), "s"(__builtin_amdgcn_readfirstlane(soff)), "s"(__builtin_amdgcn_readfirstlane(lds_dst)) : "memory", "m0");
}
#define VMWAIT(n) asm volatile("s_waitcnt vmcnt(" #n ")" ::: "memory")
#define BAR() do { asm volatile("s_waitcnt lgkmcnt(0)" ::: "memory"); __builtin_amdgcn_s_barrier(); asm volatile("" ::: "memory"); } while (0)
#define SCHED() __builtin_amdgcn_sched_barrier(0)
#define SGB(mask, n) __builtin_amdgcn_sched_group_barrier(mask, n, 0)
DI LAS char* opaque(LAS char* p) { asm volatile("" : "+v"(p)); return p; }

DI u64 mix64(u64 z) {
  z += 0x9E3779B97F4A7C15ull; z = (z ^ (z >> 30)) * 0xBF58476D1CE4E5B9ull; z = (z ^ (z >> 27)) * 0x94D049BB133111EBull;
  return z ^ (z >> 31);
}
DI void poll_flags(GAS u64* p, bool active, u64 tag) {
  for (u32 spins = 0; spins < SPIN_CAP; ++spins) {
    u64 v = tag;
    if (active) v = __hip_atomic_load(p, RLX_AGENT);
    if (__all(v == tag)) break;
    __builtin_amdgcn_s_sleep(1);
  }
  __builtin_amdgcn_fence(__ATOMIC_ACQUIRE, "wavefront");
}

DI void setp(LAS char* L, int wave, int lane, u32 step) {
  asm volatile("s_waitcnt lgkmcnt(0)" ::: "memory");
  if (lane == 0) *(volatile LAS u32*)(L + L_PROG + wave * 4) = step;
  asm volatile("" ::: "memory");
}
DI void waitp(LAS char* L, int grp, u32 step) {
  for (u32 spins = 0; spins < (1u << 24); ++spins) {
    const v4u x = *(volatile LAS v4u*)(L + L_PROG + grp * 16);
    const u32 m = min(min(x.x, x.y), min(x.z, x.w));
    if (__builtin_amdgcn_readfirstlane(m) >= step) break;
  }
  asm volatile("" ::: "memory");
}
DI void waitps(LAS char* L, int grp, u32 step) {
  for (u32 spins = 0; spins < (1u << 22); ++spins) {
    const v4u x = *(volatile LAS v4u*)(L + L_PROG + grp * 16);
    const u32 m = min(min(x.x, x.y), min(x.z, x.w));
    if (__builtin_amdgcn_readfirstlane(m) >= step) break;
    __builtin_amdgcn_s_sleep(2);
  }
  asm volatile("" ::: "memory");
}
DI void waitp1(LAS char* L, int w, u32 step) {
  for (u32 spins = 0; spins < (1u << 24); ++spins) {
    const u32 x = *(volatile LAS u32*)(L + L_PROG + w * 4);
    if (__builtin_amdgcn_readfirstlane(x) >= step) break;
  }
  asm volatile("" ::: "memory");
}

struct Params {
  const char* x; const char* wqkv; const char* wo; const char* w1; const char* w2; char* out; char* ws;
};

#define STAMP(k) do {} while (0)

#define RAMP(i, W0, W) ((W0) + 2 * (i) < (i) + (W) - 1 ? (W0) + 2 * (i) : (i) + (W) - 1)
#define ROT(x, n, r) ((x) + (r) >= (n) ? (x) + (r) - (n) : (x) + (r))
__global__ void __launch_bounds__(512, 2) fused_block(Params P) {
  extern __shared__ __attribute__((aligned(16))) char smem[];
  LAS char* L = (LAS char*)smem;
  const u32 lds0 = (u32)(uintptr_t)L;
  const int tid = threadIdx.x, lane = tid & 63;
  const int wave = __builtin_amdgcn_readfirstlane(tid >> 6);
  const int bid = blockIdx.x;
  const int gi = (bid & 7) * 2 + (bid >> 7);
  const int gj = (bid >> 3) & 15;
  const int tok = lane & 15, g4 = lane >> 4, q4 = (lane & 15) >> 2, p4 = lane & 3;
  const bool isM = wave < 4;
  const int v = wave & 3;
  const __amdgpu_buffer_rsrc_t rs = __builtin_amdgcn_make_buffer_rsrc((void*)P.ws, 0, WS_END, 0x00020000);
  GAS u64* flags = (GAS u64*)(P.ws + WS_FLAGS);
  GAS u64* inbox = (GAS u64*)(P.ws + WS_INBOX);
  const int me = gi * 16 + gj;
  const int ti = ((bid & 7) >> 1) * 4 + ((bid >> 3) & 3), tj = (bid & 1) * 8 + (bid >> 5);
  const int rot = (ti + tj) % 10;
  const int l16 = lane * 16;
  const bool isX = wave >= 6;
  const int lv = wave & 1, D = isX ? 4 : 5;
  const v4i rl = isX ? mkrsrc(P.x, 2048 * 1280) : mkrsrc(P.wqkv, 640 * 3840);
  u32 vo[2];
#pragma unroll
  for (int k = 0; k < 2; ++k)
    vo[k] = isX ? (lane >> 3) * 1280 + (((lane & 7) ^ (lane >> 3)) * 16) : g4 * 3840 + (((tok >> 1) ^ (g4 | (k << 2))) * 32) + (tok & 1) * 16;
  const u32 sb = isX ? (128 * ti + 64 * lv) * 1280 : tj * 256 + (32 * lv) * 3840;
  const u32 cstr = isX ? 128 : 64 * 3840, pstr = isX ? 8 * 1280 : 4 * 3840;
  const u32 db = lds0 + (isX ? L_XR : L_RING) + lv * 8192;
  int slot = 0, kc = rot;
#define L_ISSUE() { _Pragma("unroll") for (int e = 0; e < 8; ++e) dmab(rl, vo[(e >> 1) & 1], sb + kc * cstr + e * pstr, db + slot * 16384 + e * 1024); \
    slot = slot + 1 == D ? 0 : slot + 1; kc = kc == 9 ? 0 : kc + 1; }
  if (!isM && tj != 15) L_ISSUE()
  const int rh = gj >> 3, ct = gj & 7;
  const int T0 = gj < 12 ? 7 * gj : 84 + 6 * (gj - 12);
  const int nT = gj < 12 ? 7 : 6;
  const int r5 = (ct * 30) >> 3;
  const int r3 = 2 * ct, r4 = (gj * 5) >> 2;
#define KS3(p) ((p) < 16 ? (((p) + r3) & 15) : (p))
#define KS4(s) ((s) + r4 >= 20 ? (s) + r4 - 20 : (s) + r4)
#define KS5(s) ((s) < 30 ? ((s) + r5 >= 30 ? (s) + r5 - 30 : (s) + r5) : (s))
  const v4u ones = {0x3F803F80u, 0x3F803F80u, 0x3F803F80u, 0x3F803F80u};
  const f32x4 zero = {0.f, 0.f, 0.f, 0.f};
  STAMP(0);
  {
    u64 pc0; u32 len;
    asm volatile("s_getpc_b64 %0\n\ts_mov_b32 %1, fb_code_end-." : "=s"(pc0), "=s"(len));
    if (wave == CODE_PF && len <= 131072u) {
      const u32 per = ((len >> 5) + 1023u) & ~1023u;
      const v4i rc = mkrsrc((const void*)(pc0 & ~63ull), (int)len);
      for (u32 o = (bid >> 3) * per; o < ((bid >> 3) + 1) * per; o += 1024) dmab(rc, o + l16, 0, lds0 + L_ST1);
    }
  }
  if (tid < 8) *(volatile LAS u32*)(L + L_PROG + tid * 4) = 0u;
  const v4i rsi = mkrsrc(P.ws, WS_END);
  if (wave == 0) dmas(rsi, l16, 0, lds0 + L_EP);
  u64 tag;
#define GET_TAG() { const v2u ep = *(volatile LAS v2u*)(L + L_EP); \
    tag = mix64((u64)__builtin_amdgcn_readfirstlane(ep.x) | ((u64)__builtin_amdgcn_readfirstlane(ep.y) << 32)); }
#define MAKE_TAG() { VMWAIT(0); GET_TAG() }
  v4u xv[10];
  if (wave == 0) {
    const GAS char* xr = (const GAS char*)P.x + (size_t)(128 * gi + 8 * gj + (lane >> 3)) * 1280 + (lane & 7) * 16;
#pragma unroll
    for (int e = 0; e < 10; ++e) xv[e] = *(const GAS v4u*)(xr + e * 128);
    SCHED();
  }
#define ROW_STATS() if (wave == 0) { \
    float s1 = 0.f, s2 = 0.f; \
    _Pragma("unroll") for (int e = 0; e < 10; ++e) _Pragma("unroll") for (int k = 0; k < 4; ++k) { s1 = dot2(xv[e][k], 0x3F803F80u, s1); s2 = dot2(xv[e][k], xv[e][k], s2); } \
    _Pragma("unroll") for (int o = 1; o < 8; o <<= 1) { s1 += __shfl_xor(s1, o); s2 += __shfl_xor(s2, o); } \
    const float mean = s1 * (1.f / 640.f); \
    const float var = fmaxf(s2 * (1.f / 640.f) - mean * mean, 0.f); \
    const float rstd = __builtin_amdgcn_rcpf(rbf(__builtin_amdgcn_sqrtf(rbf(rbf(var) + 1e-5f)))); \
    if ((lane & 7) == 0) \
      __builtin_amdgcn_raw_buffer_store_b64(v2u{__float_as_uint(rbf(mean)), __float_as_uint(rstd)}, rs, (lane >> 3) * 8, WS_ST0 + (128 * gi + 8 * gj) * 8, AUX_SC1); }
#define ROW_STATS_FLAG() if (wave == 0) { MAKE_TAG() if (lane == 0) __hip_atomic_store(flags + (5 * 16 + gi) * 16 + gj, tag, RLX_AGENT); }
  bool f1act; GAS u64* f1dst;
  {
    const int sec = tj / 5, jj = tj - 5 * sec, a = lane >> 2, g = (32 * jj) / 10 + (lane & 3), ci = ti + a;
    f1act = lane < 12 && tj != 15 && ci < 16 && (sec != 0 || a == 0);
      const int slot = 2 * (sec == 0 ? 0 : (sec == 1 ? 3 : 6) - a) + (((10 * g) >> 5) == jj ? 0 : 1);
    f1dst = f1act ? inbox + (ci * 16 + g) * 16 + slot : flags + ti * 16 + tj;
    asm volatile("" : "+v"(f1dst));
  }
  if (tj == 15) {
    const int R = ti * 8 + wave;
    LAS char* stg = L + wave * 4096;
    const int srow = lane >> 3, sc = (lane & 7) * 16;
    LAS char* tb = stg + (4 * g4 + q4) * 128 + p4 * 8;
    v4u d[3][4];
    const __amdgpu_buffer_rsrc_t rwo = __builtin_amdgcn_make_buffer_rsrc((void*)P.wo, 0, 640 * 1280, 0x00020000);
    const __amdgpu_buffer_rsrc_t rw1 = __builtin_amdgcn_make_buffer_rsrc((void*)P.w1, 0, 640 * 3456, 0x00020000);
#pragma unroll
    for (int u = 0; u < 3; ++u) {
      const int U = R + 128 * u;
      if (u < 2 && U < 200) {
        const int ks = U / 10, lg = U - ks * 10;
#pragma unroll
        for (int e = 0; e < 4; ++e) {
          const int rho = 8 * e + srow, t = rho >> 4, gg = (rho >> 2) & 3, qq = rho & 3;
          const int row = ks < 16 ? (2 * ks + (gg >> 1)) * 20 + 8 * t + 4 * (gg & 1) + qq : (2 * (4 * (ks - 16) + gg) + t) * 20 + 16 + qq;
          d[u][e] = __builtin_amdgcn_raw_buffer_load_b128(rwo, row * 1280 + sc, lg * 128, 0);
        }
      } else {
        const int U1 = U - 200, ks = U1 / 27, cg = U1 - ks * 27;
#pragma unroll
        for (int e = 0; e < 4; ++e) d[u][e] = __builtin_amdgcn_raw_buffer_load_b128(rw1, (8 * e + srow) * 3456 + sc, ks * (32 * 3456) + cg * 128, 0);
      }
    }
    SCHED();
    BAR();
    ROW_STATS()
    ROW_STATS_FLAG()
#pragma unroll
    for (int u = 0; u < 3; ++u) {
      const int U = R + 128 * u;
#pragma unroll
      for (int e = 0; e < 4; ++e) *(LAS v4u*)(stg + e * 1024 + l16) = d[u][e];
      if (u < 2 && U < 200) {
        const int ks = U / 10, lg = U - ks * 10;
#pragma unroll
        for (int n = 0; n < 4; ++n)
          __builtin_amdgcn_raw_buffer_store_b128(trfrag(tb + n * 32, tb + n * 32 + 2048), rs, l16, WS_WOT + (ks * 40 + 4 * lg + n) * 1024, AUX_SC1);
      } else {
        const int U1 = U - 200, ks = U1 / 27, cg = U1 - ks * 27;
        f32x4 c4[4];
#pragma unroll
        for (int n = 0; n < 4; ++n) {
          const v4u f = trfrag(tb + n * 32, tb + n * 32 + 2048);
          __builtin_amdgcn_raw_buffer_store_b128(f, rs, l16, WS_W1T + (ks * 108 + 4 * cg + n) * 1024, AUX_SC1);
          c4[n] = mfma(f, ones, zero);
        }
        const f32x4 cv = tok == 0 ? c4[0] : (tok == 1 ? c4[1] : (tok == 2 ? c4[2] : c4[3]));
        if (tok < 4) __builtin_amdgcn_raw_buffer_store_b128(__builtin_bit_cast(v4u, cv), rs, tok * 64 + g4 * 16, WS_CS1 + (ks * 1792 + cg * 64) * 4, AUX_SC1);
      }
      if (u == 1) {
        VMWAIT(0);
        BAR();
        GET_TAG()
        if (tid == 0) __hip_atomic_store(flags + (4 * 16 + 0) * 16 + ti, tag, RLX_AGENT);
      }
    }
    VMWAIT(0);
    STAMP(1);
    BAR();
    if (tid == 0) __hip_atomic_store(flags + (4 * 16 + 1) * 16 + ti, tag, RLX_AGENT);
    if (isM) setp(L, wave, lane, 13);
  } else if (isM) {
    BAR();
    ROW_STATS()
    const int mh = wave >> 1, nh = wave & 1;
    f32x4 acc1[4][4];
#pragma unroll
    for (int m = 0; m < 4; ++m)
#pragma unroll
      for (int n = 0; n < 4; ++n) acc1[m][n] = zero;
    f32x4 cs[2] = {zero, zero};
    LAS char* bn[4];
#pragma unroll
    for (int n = 0; n < 4; ++n) bn[n] = L + L_RING + (8 * g4 + q4) * 256 + (((4 * nh + (n ^ (2 * mh))) ^ (q4 | ((g4 & 1) << 2))) * 32) + p4 * 8;
    LAS char* an[2];
#pragma unroll
    for (int k = 0; k < 2; ++k) an[k] = L + L_XR + (64 * mh + tok) * 128 + (((4 * k + g4) ^ (tok & 7)) * 16);
    v4u b[2][4], a[2][4], pg;
    u64 sfl; v2u stv[4];
    waitp(L, 1, 1);
#pragma unroll
    for (int n = 0; n < 4; ++n) { b[0][n] = trfrag(bn[n], bn[n] + 1024); a[0][n] = *(LAS v4u*)(an[0] + n * 2048); }
#pragma unroll
    for (int s = 0; s < 20; ++s) {
      if (s + 1 < 20) {
        const int c1 = (s + 1) >> 1, k1 = (s + 1) & 1, sl = c1 % 5;
        if (k1 == 1) pg = *(volatile LAS v4u*)(L + L_PROG + 16);
        if (k1 == 0) {
          if (__builtin_amdgcn_readfirstlane(min(min(pg.x, pg.y), min(pg.z, pg.w))) < (u32)(c1 + 1)) waitp(L, 1, c1 + 1);
          asm volatile("" ::: "memory");
          if (sl == 4 || sl == 0) {
#pragma unroll
            for (int n = 0; n < 4; ++n) bn[n] = opaque(bn[n] + (sl == 4 ? 65536 : -65536));
          }
        }
        const int so = (sl & 3) * 16384 + k1 * 8192;
#pragma unroll
        for (int n = 0; n < 4; ++n) {
          b[(s + 1) & 1][n] = trfrag(bn[n] + so, bn[n] + so + 1024);
          a[(s + 1) & 1][n] = *(LAS v4u*)(an[k1] + (c1 & 3) * 16384 + n * 2048);
        }
      }
#pragma unroll
      for (int n = 0; n < 4; ++n)
#pragma unroll
        for (int m = 0; m < 4; ++m) acc1[m][n] = mfma(b[s & 1][n], a[s & 1][m], acc1[m][n]);
      cs[0] = mfma(b[s & 1][0], ones, cs[0]); cs[1] = mfma(b[s & 1][1], ones, cs[1]);
#pragma unroll
      for (int n = 0; n < 13; ++n) { SGB(0x8, 1); SGB(0x100, 1); }
      SCHED();
      if ((s & 1) == 1 && s < 19 && lane == 0) *(volatile LAS u32*)(L + L_PROG + wave * 4) = (u32)(s >> 1) + 1u;
      if (s == 3) ROW_STATS_FLAG()
      if (s == 11) sfl = __hip_atomic_load(flags + (5 * 16 + ti) * 16 + tok, RLX_AGENT);
      if (s == 15) {
        GET_TAG()
        if (!__all(sfl == tag)) poll_flags(flags + (5 * 16 + ti) * 16 + tok, true, tag);
        __builtin_amdgcn_fence(__ATOMIC_ACQUIRE, "wavefront");
#pragma unroll
        for (int m = 0; m < 4; ++m) stv[m] = __builtin_amdgcn_raw_buffer_load_b64(rs, (16 * m + tok) * 8, WS_ST0 + (128 * ti + 64 * mh) * 8, AUX_SC1);
      }
    }
    if (tok == 0) {
      *(LAS f32x4*)(L + L_CS + (64 * nh + 32 * mh + 4 * g4) * 4) = cs[0];
      *(LAS f32x4*)(L + L_CS + (64 * nh + 32 * mh + 16 + 4 * g4) * 4) = cs[1];
    }
    setp(L, wave, lane, 10);
    STAMP(1);
    const int sec = tj / 5, jj = tj - 5 * sec;
    const float qs = sec == 0 ? 0.22360679774997896f * 1.4426950408889634f : 1.f;
    float mb[4], rstd[4];
#pragma unroll
    for (int m = 0; m < 4; ++m) {
      mb[m] = __uint_as_float(stv[m].x); rstd[m] = __uint_as_float(stv[m].y) * qs;
    }
    waitp(L, 0, 10);
    const int so = WS_IMG + ((sec * 128 + 8 * ti + 4 * mh) * 80 + 16 * jj) * 256;
    const int vo = (2 * (g4 & 1) + (g4 >> 1)) * 256 + tok * 16;
#pragma unroll
    for (int e = 0; e < 2; ++e) {
      const int tp = 2 * nh + (e ^ mh);
      const f32x4 c0 = *(LAS f32x4*)(L + L_CS + (32 * tp + 4 * g4) * 4), c1 = *(LAS f32x4*)(L + L_CS + (32 * tp + 16 + 4 * g4) * 4);
#pragma unroll
      for (int m = 0; m < 4; ++m) {
        const f32x4 x = acc1[m][2 * e], y = acc1[m][2 * e + 1];
        const u32 x0 = pk2((x[0] - mb[m] * c0[0]) * rstd[m], (x[1] - mb[m] * c0[1]) * rstd[m]);
        const u32 x1 = pk2((x[2] - mb[m] * c0[2]) * rstd[m], (x[3] - mb[m] * c0[3]) * rstd[m]);
        const u32 y0 = pk2((y[0] - mb[m] * c1[0]) * rstd[m], (y[1] - mb[m] * c1[1]) * rstd[m]);
        const u32 y1 = pk2((y[2] - mb[m] * c1[2]) * rstd[m], (y[3] - mb[m] * c1[3]) * rstd[m]);
        auto r0 = __builtin_amdgcn_permlane16_swap(x0, y0, false, false);
        auto r1 = __builtin_amdgcn_permlane16_swap(x1, y1, false, false);
        __builtin_amdgcn_raw_buffer_store_b128(v4u{r0[0], r1[0], r0[1], r1[1]}, rs, vo, so + m * 20480 + tp * 1024, AUX_SC1);
      }
    }
    STAMP(14);
    VMWAIT(0);
    setp(L, wave, lane, 13);
  } else {
    BAR();
#pragma unroll 1
    for (int c = 1; c < 10; ++c) {
      if (c >= D) waitp(L, 0, c - D + 1);
      L_ISSUE()
      if (c < 9) { VMWAIT(8); if (lane == 0) *(volatile LAS u32*)(L + L_PROG + wave * 4) = (u32)c; }
    }
    const int U = (ti * 15 + tj) * 4 + v;
    const bool isW2 = U < 540, hasU = U < 896;
    const int uks = isW2 ? U / 10 : (hasU ? (U - 356) / 27 : 0), ulg = isW2 ? U - uks * 10 : (hasU ? (U - 356) - uks * 27 : 0);
    {
      const v4i ru = isW2 ? mkrsrc(P.w2, 1728 * 1280) : mkrsrc(P.w1, 640 * 3456);
      const int rstr = isW2 ? 1280 : 3456;
      const u32 sd = lds0 + (v < 3 ? L_UNIT + v * 4096 : L_XR + 2 * 16384);
      if (v == 3) waitp(L, 0, 7);
#pragma unroll
      for (int e = 0; e < 4; ++e) dmab(ru, (8 * e + (lane >> 3)) * rstr + (lane & 7) * 16, uks * 32 * rstr + ulg * 128, sd + e * 1024);
      VMWAIT(12); if (lane == 0) *(volatile LAS u32*)(L + L_PROG + wave * 4) = 9u;
      VMWAIT(4); if (lane == 0) *(volatile LAS u32*)(L + L_PROG + wave * 4) = 10u;
      VMWAIT(0);
      if (hasU) {
        LAS char* tb = L + (sd - lds0) + (4 * g4 + q4) * 128 + p4 * 8;
        const int dst = isW2 ? WS_W2T + (uks * 40 + 4 * ulg) * 1024 : WS_W1T + (uks * 108 + 4 * ulg) * 1024;
        f32x4 c4[4];
#pragma unroll
        for (int n = 0; n < 4; ++n) {
          const v4u f = trfrag(tb + n * 32, tb + n * 32 + 2048);
          __builtin_amdgcn_raw_buffer_store_b128(f, rs, l16, dst + n * 1024, AUX_SC1);
          c4[n] = mfma(f, ones, zero);
        }
        const f32x4 cv = tok == 0 ? c4[0] : (tok == 1 ? c4[1] : (tok == 2 ? c4[2] : c4[3]));
        if (tok < 4 && !isW2) __builtin_amdgcn_raw_buffer_store_b128(__builtin_bit_cast(v4u, cv), rs, tok * 64 + g4 * 16, WS_CS1 + (uks * 1792 + ulg * 64) * 4, AUX_SC1);
      }
    }
    VMWAIT(0);
    setp(L, wave, lane, 12);
    STAMP(1);
  }
  STAMP(2);
  const int hh = wave & 1, qg = wave >> 1;
  u64 fl[5];
  if (wave == 0) {
    {
      waitp(L, 0, 13);
      if (tj != 15) waitp(L, 1, 12);
      if (f1act || lane == 12) __hip_atomic_store(f1dst, tag, RLX_AGENT);
    }
    STAMP(15);
  }
  int go[8], lo[8];
  {
    const int isV = wave >> 2, t0 = 6 * (wave & 3);
    const int tmin = 16 - 8 * gi - t0;
#pragma unroll
    for (int e = 0; e < 8; ++e) {
      const int x = e * 64 + lane, t4 = x / 80, rem = x - t4 * 80;
      const bool ok = x < 480 && t4 >= tmin;
      lo[e] = ok ? (isV ? L_V + (t0 + t4) * V_TS + (rem >> 4) * 320 + tok * 16 : L_K + t0 * 1280 + x * 16) : L_CS + (lane & 15) * 16;
      go[e] = ok ? t4 * 20480 + rem * 16 : 0x7FFFFFF0;
    }
  }
  const int qo = ((8 * gi + 2 * qg + ((lane & 31) >> 4)) * 80 + 5 * gj) * 256 + tok * 16;
  const int r32 = lane & 31, h = lane >> 5, dg = (lane >> 4) & 1;
  const float NEG = -__builtin_inff();
  LAS char* k1p = L + L_K + (2 * qg + (r32 >> 4)) * 1280 + (r32 & 15) * 16;
  LAS char* k0p = k1p + (3 * hh + h) * 256;
  k1p += 512;
  LAS char* vp[2];
#pragma unroll
  for (int t = 0; t < 2; ++t) {
    const int key = 8 * t + 4 * h + q4, gam = 5 * hh + (dg ? 4 : p4);
    const int off = (dg && p4) ? 1600 : (gam >> 1) * 320 + key * 16 + (gam & 1) * 8;
    vp[t] = L + L_V + (2 * qg) * V_TS + off;
  }
  k0p = opaque(k0p); k1p = opaque(k1p); vp[0] = opaque(vp[0]); vp[1] = opaque(vp[1]);
  if (wave == 0) {
    const int slot = lane >> 1;
    const int row = slot == 0 ? gi : gi - 2 + (slot < 4 ? slot - 1 : slot - 4);
    const bool act = lane < 14 && row >= 0 && ((lane & 1) == 0 || ((10 * gj) >> 5) != ((10 * gj + 9) >> 5));
    poll_flags(inbox + me * 16 + (lane & 15), act, tag);
    waitp1(L, 4, 13);
    setp(L, wave, lane, 14);
  } else if (!isM) {
    if (wave == 4) {
      GET_TAG()
      poll_flags(flags + (4 * 16 + 0) * 16 + tok, lane < 16, tag);
      setp(L, wave, lane, 13);
    } else waitp1(L, 4, 13);
    waitp(L, 0, 13);
    const int wrot = 22 * (2 * (gi & 1) + rh);
    v4u wo_[23];
#define WOU(e) ROT((v + 4 * (e) > 89 ? 89 : v + 4 * (e)), 90, wrot)
#pragma unroll
    for (int e = 0; e < 23; ++e) { const int u = WOU(e), ks = u / 5;
      wo_[e] = __builtin_amdgcn_raw_buffer_load_b128(rs, l16, WS_WOT + ct * 5120 + KS3(ks) * 40960 + (u - 5 * ks) * 1024, AUX_SC1); }
    SCHED();
#pragma unroll
    for (int e = 0; e < 23; ++e) *(LAS v4u*)(L + L_WO + WOU(e) * 1024 + l16) = wo_[e];
    waitp1(L, 0, 14);
  } else waitp1(L, 0, 14);
  STAMP(3);
  v4u q0, q1;
  {
    const int gb = WS_IMG + (((1 + (wave >> 2)) * 128 + 8 * gi - 16 + 6 * (wave & 3)) * 80 + 5 * gj) * 256;
    v4u hv[8];
#pragma unroll
    for (int e = 0; e < 8; ++e) hv[e] = __builtin_amdgcn_raw_buffer_load_b128(rs, go[e], gb, AUX_SC1);
    q0 = __builtin_amdgcn_raw_buffer_load_b128(rs, qo + (3 * hh + (lane >> 5)) * 256, WS_IMG, AUX_SC1);
    q1 = __builtin_amdgcn_raw_buffer_load_b128(rs, qo + 512, WS_IMG, AUX_SC1);
    if (wave == 7) {
#pragma unroll
      for (int r = 0; r < 4; ++r) fl[r] = __hip_atomic_load(flags + r * 64 + lane, RLX_AGENT);
      fl[4] = __hip_atomic_load(flags + (4 * 16 + 1) * 16 + (lane & 15), RLX_AGENT);
    }
    SCHED();
    STAMP(16);
    if (wave == 2 && lane < 24) *(LAS v4u*)(L + L_V + lane * V_TS + 1600) = ones;
#pragma unroll
    for (int e = 0; e < 8; ++e) *(LAS v4u*)(L + lo[e]) = hv[e];
    GET_TAG()
    if (isM) {
      float km = 0.f;
#pragma unroll
      for (int e = 0; e < 8; ++e) km = fmaxf(km, dot2(hv[e].x, hv[e].x, dot2(hv[e].y, hv[e].y, dot2(hv[e].z, hv[e].z, dot2(hv[e].w, hv[e].w, 0.f)))));
      km = km == km ? km : __builtin_inff();
      km = fmaxf(km, __int_as_float(__builtin_amdgcn_update_dpp(0, __float_as_int(km), 0x128, 0xf, 0xf, false)));
      km = fmaxf(km, __int_as_float(__builtin_amdgcn_update_dpp(0, __float_as_int(km), 0x124, 0xf, 0xf, false)));
      km = fmaxf(km, __int_as_float(__builtin_amdgcn_update_dpp(0, __float_as_int(km), 0x122, 0xf, 0xf, false)));
      km = fmaxf(km, __int_as_float(__builtin_amdgcn_update_dpp(0, __float_as_int(km), 0x121, 0xf, 0xf, false)));
      km = max4(km);
      if (lane == 0) *(LAS float*)(L + L_PROG + 32 + wave * 4) = km;
    }
    if (wave == 7) {
      const bool ok = __all(fl[0] == tag && fl[1] == tag && fl[2] == tag && fl[3] == tag && fl[4] == tag);
      if (lane == 0) *(LAS u32*)(L + L_PROG + 48) = ok ? 1u : 0u;
    }
  }
  if (lane >= 32) q1 = v4u{0u, 0u, 0u, 0u};
  else if (hh == 0) { q1.z = 0u; q1.w = 0u; }
  else { q1.x = 0u; q1.y = 0u; }
  BAR();
  STAMP(4);
  {
    f32x16 o, z16;
#pragma unroll
    for (int i = 0; i < 16; ++i) { o[i] = 0.f; z16[i] = 0.f; }
    const int dq = r32 - 4 * h;
#define UP(i) (((i) & 3) + 8 * ((i) >> 2) > dq)
#define QK(b, c) mfma32(*(LAS v4u*)(k1p + (b) * 2560), q1, mfma32(*(LAS v4u*)(k0p + (b) * 2560), q0, c))
#define MASK_EDGE(s) _Pragma("unroll") for (int i = 0; i < 16; ++i) s[i] = UP(i) ? s[i] : NEG
#define MASK_DIAG(s) _Pragma("unroll") for (int i = 0; i < 16; ++i) s[i] = UP(i) ? NEG : s[i]
#define MAXS(s) _Pragma("unroll") for (int i = 0; i < 16; i += 4) { m = max3(m, s[i], s[i + 1]); m2 = max3(m2, s[i + 2], s[i + 3]); }
#define EX(x) __builtin_amdgcn_exp2f(x)
#define PV(b, s) { const v4u pa = {pk2(EX(s[0]), EX(s[1])), pk2(EX(s[2]), EX(s[3])), pk2(EX(s[4]), EX(s[5])), pk2(EX(s[6]), EX(s[7]))}; \
      const v4u pb = {pk2(EX(s[8]), EX(s[9])), pk2(EX(s[10]), EX(s[11])), pk2(EX(s[12]), EX(s[13])), pk2(EX(s[14]), EX(s[15]))}; \
      o = mfma32(trfrag(vp[0] + (b) * (2 * V_TS), vp[1] + (b) * (2 * V_TS)), pa, o); o = mfma32(trfrag(vp[0] + (b) * (2 * V_TS) + V_TS, vp[1] + (b) * (2 * V_TS) + V_TS), pb, o); }
    if (!isM) __builtin_amdgcn_s_setprio(1);
    const bool flok = __builtin_amdgcn_readfirstlane(*(LAS u32*)(L + L_PROG + 48)) != 0u;
    if (!flok) {
      poll_flags(flags + (4 * 16 + 1) * 16 + (lane & 15), true, tag);
#pragma unroll 1
      for (int r = 0; r < 4; ++r) poll_flags(flags + r * 64 + lane, true, tag);
    }
    if (bid == 0 && tid == 0) __hip_atomic_store((GAS u64*)(P.ws + WS_EPOCH), tag, RLX_AGENT);
    const int bmin = 8 - 4 * gi - qg;
    float m;
    {
      const f32x4 km4 = *(LAS f32x4*)(L + L_PROG + 32);
      float qn = dot2(q0.x, q0.x, dot2(q0.y, q0.y, dot2(q0.z, q0.z, dot2(q0.w, q0.w, dot2(q1.x, q1.x, dot2(q1.y, q1.y, dot2(q1.z, q1.z, dot2(q1.w, q1.w, 0.f))))))));
      auto r = __builtin_amdgcn_permlane32_swap(__float_as_uint(qn), __float_as_uint(qn), false, false);
      qn = __uint_as_float(r[0]) + __uint_as_float(r[1]);
      m = 1.01f * __builtin_amdgcn_sqrtf(qn * 3.f * max3(fmaxf(km4[0], km4[1]), km4[2], km4[3])) + 0.01f;
    }
    if (!__all(m <= 40.f)) {
      float m2 = NEG;
      m = NEG;
      { f32x16 s = QK(8, z16); MASK_DIAG(s); MAXS(s) }
      if (bmin <= 0) { f32x16 s = QK(0, z16); MASK_EDGE(s); MAXS(s) }
#pragma unroll 1
      for (int b = bmin > 1 ? bmin : 1; b < 8; ++b) { f32x16 s = QK(b, z16); MAXS(s) }
      m = max3(m, m2, m2);
      auto r = __builtin_amdgcn_permlane32_swap(__float_as_uint(m), __float_as_uint(m), false, false);
      m = max3(m, __uint_as_float(r[0]), __uint_as_float(r[1]));
    }
    f32x16 nm;
#pragma unroll
    for (int i = 0; i < 16; ++i) nm[i] = -m;
    if (bmin <= 0) {
      f32x16 s = QK(0, nm);
#pragma unroll
      for (int b = 0; b < 9; ++b) {
        f32x16 sn = s;
        if (b < 8) sn = QK(b + 1, nm);
        if (b == 0) MASK_EDGE(s);
        if (b == 8) MASK_DIAG(s);
        PV(b, s)
        s = sn;
      }
    } else {
      { f32x16 s = QK(8, nm); MASK_DIAG(s); PV(8, s) }
#pragma unroll 1
      for (int b = bmin; b < 8; ++b) { f32x16 s = QK(b, nm); PV(b, s) }
    }
    const float inv = __builtin_amdgcn_rcpf(o[12]);
    const int mt = 2 * qg + (r32 >> 4);
    const int ob = WS_OF + (gi * 8) * 20 * 1024;
    __builtin_amdgcn_raw_buffer_store_b128(
        v4u{pk2(o[0] * inv, o[1] * inv), pk2(o[2] * inv, o[3] * inv), pk2(o[4] * inv, o[5] * inv), pk2(o[6] * inv, o[7] * inv)}, rs,
        mt * 20480 + ((2 * hh + h) * 16 + tok) * 16, ob + gj * 1024, AUX_SC1);
    if (h == 0)
      __builtin_amdgcn_raw_buffer_store_b64(v2u{pk2(o[8] * inv, o[9] * inv), pk2(o[10] * inv, o[11] * inv)}, rs,
          mt * 20480 + tok * 16, ob + (16 + (gj >> 2)) * 1024 + (gj & 3) * 256 + 8 * hh, AUX_SC1);
  }
  __builtin_amdgcn_s_setprio(0);
  VMWAIT(0);
  BAR();
  STAMP(5);
  const int mt3 = 4 * rh + v;
  if (isM) {
    u32 hres[10];
    if (wave == 0 && lane < 16) __hip_atomic_store(inbox + (1 * 256 + gi * 16 + lane) * 16 + gj, tag, RLX_AGENT);
    v2u xres[5];
    const GAS char* xr = (const GAS char*)P.x + (size_t)(128 * gi + 16 * mt3 + tok) * 1280 + ct * 160 + g4 * 8;
#pragma unroll
    for (int t = 0; t < 5; ++t) xres[t] = *(const GAS v2u*)(xr + t * 32);
    if (wave == 0) { poll_flags(inbox + (1 * 256 + me) * 16 + (lane & 15), lane < 16, tag); setp(L, wave, lane, 17); }
    else waitp1(L, 0, 17);
    STAMP(6);
    {
      v4u a[20];
      const int ab = WS_OF + (gi * 8 + mt3) * 20 * 1024;
#pragma unroll
      for (int s = 0; s < 20; ++s) { const int ks = KS3(s); a[s] = __builtin_amdgcn_raw_buffer_load_b128(rs, l16, ab + ks * 1024, AUX_SC1); }
      SCHED();
      setp(L, wave, lane, 18);
      STAMP(17);
      f32x4 acc[5];
#pragma unroll
      for (int n = 0; n < 5; ++n) acc[n] = zero;
      LAS char* bb = L + L_WO + l16;
      LAS char* bh = opaque(bb + 12 * 5120);
      v4u bf[3][5];
#pragma unroll
      for (int k = 0; k < 2; ++k) {
        LAS char* b = bb + k * 5120;
#pragma unroll
        for (int n = 0; n < 5; ++n) bf[k][n] = *(LAS v4u*)(b + n * 1024);
      }
#pragma unroll
      for (int s = 0; s < 20; ++s) {
        if (s == 16) waitp(L, 1, 14);
        if (s + 2 < 20) {
          LAS char* b = s + 2 < 12 ? bb + (s + 2) * 5120 : bh + (s + 2 - 12) * 5120;
#pragma unroll
          for (int n = 0; n < 5; ++n) bf[(s + 2) % 3][n] = *(LAS v4u*)(b + n * 1024);
        }
#pragma unroll
        for (int n = 0; n < 5; ++n) acc[n] = mfma(bf[s % 3][n], a[s], acc[n]);
#pragma unroll
        for (int n = 0; n < 5; ++n) { SGB(0x8, 1); SGB(0x100, 1); }
        SCHED();
      }
      setp(L, wave, lane, 19);
      STAMP(18);
      float s1 = 0.f, s2 = 0.f;
#pragma unroll
      for (int n = 0; n < 5; ++n) {
        const float h0 = bflo(xres[n].x) + rbf(acc[n][0]), h1 = bfhi(xres[n].x) + rbf(acc[n][1]);
        const float h2 = bflo(xres[n].y) + rbf(acc[n][2]), h3 = bfhi(xres[n].y) + rbf(acc[n][3]);
        hres[2 * n] = pk2(h0, h1); hres[2 * n + 1] = pk2(h2, h3);
        s1 = dot2(hres[2 * n], 0x3F803F80u, s1); s1 = dot2(hres[2 * n + 1], 0x3F803F80u, s1);
        s2 = dot2(hres[2 * n], hres[2 * n], s2); s2 = dot2(hres[2 * n + 1], hres[2 * n + 1], s2);
      }
      const int hb = WS_HF + (gi * 8 + mt3) * 20 * 1024 + ((5 * ct) >> 1) * 1024;
      if ((ct & 1) == 0) {
        __builtin_amdgcn_raw_buffer_store_b128(v4u{hres[0], hres[1], hres[2], hres[3]}, rs, l16, hb, AUX_SC1);
        __builtin_amdgcn_raw_buffer_store_b128(v4u{hres[4], hres[5], hres[6], hres[7]}, rs, l16, hb + 1024, AUX_SC1);
        __builtin_amdgcn_raw_buffer_store_b64(v2u{hres[8], hres[9]}, rs, l16, hb + 2048, AUX_SC1);
      } else {
        __builtin_amdgcn_raw_buffer_store_b64(v2u{hres[0], hres[1]}, rs, l16 + 8, hb, AUX_SC1);
        __builtin_amdgcn_raw_buffer_store_b128(v4u{hres[2], hres[3], hres[4], hres[5]}, rs, l16, hb + 1024, AUX_SC1);
        __builtin_amdgcn_raw_buffer_store_b128(v4u{hres[6], hres[7], hres[8], hres[9]}, rs, l16, hb + 2048, AUX_SC1);
      }
      s1 = sum4(s1); s2 = sum4(s2);
      if (g4 == 0)
        __builtin_amdgcn_raw_buffer_store_b64(v2u{__float_as_uint(s1), __float_as_uint(s2)}, rs,
            WS_ST + (128 * gi + 16 * mt3 + tok) * 64 + ct * 8, 0, AUX_SC1);
      setp(L, wave, lane, 20);
      VMWAIT(0);
      setp(L, wave, lane, 21);
    }
    STAMP(7);
    if (wave == 0) {
      waitp(L, 0, 21);
      if (lane < 16) __hip_atomic_store(inbox + (2 * 256 + gi * 16 + lane) * 16 + gj, tag, RLX_AGENT);
      poll_flags(inbox + (2 * 256 + me) * 16 + (lane & 15), lane < 16, tag);
      setp(L, wave, lane, 22);
    } else waitp1(L, 0, 22);
    STAMP(8);
    {
      constexpr int W = 10, W0 = 4;
      v4u a[W][2];
      const int ab = WS_HF + (gi * 8 + 2 * wave) * 20 * 1024;
#define A4LD(s) { a[(s) % W][0] = __builtin_amdgcn_raw_buffer_load_b128(rs, l16, ab + KS4(s) * 1024, AUX_SC1); \
        a[(s) % W][1] = __builtin_amdgcn_raw_buffer_load_b128(rs, l16, ab + (20 + KS4(s)) * 1024, AUX_SC1); }
#pragma unroll
      for (int s = 0; s < W0; ++s) A4LD(s)
      v4u stv[2];
#pragma unroll
      for (int m = 0; m < 2; ++m)
        stv[m] = __builtin_amdgcn_raw_buffer_load_b128(rs, WS_ST + (128 * gi + 32 * wave + 16 * m + tok) * 64 + g4 * 16, 0, AUX_SC1);
      SCHED();
      f32x4 acc[2][7];
#pragma unroll
      for (int m = 0; m < 2; ++m)
#pragma unroll
        for (int n = 0; n < 7; ++n) acc[m][n] = zero;
      LAS char* bb = L + l16;
      LAS char* bA = opaque(bb + L_W1A);
      LAS char* bC = opaque(bb + 8 * 7168);
#define W1P(p) ((p) < 8 ? bA + (p) * 7168 : ((p) < 16 ? bb + ((p) - 8) * 7168 : bC + ((p) - 16) * 7168))
      waitp(L, 1, 15);
      STAMP(19);
      float mb2[2], rs2[2];
      v4u bf[2][7];
      {
        LAS char* b = W1P(0);
#pragma unroll
        for (int n = 0; n < 7; ++n) bf[0][n] = *(LAS v4u*)(b + n * 1024);
      }
#pragma unroll
      for (int s = 0; s < 20; ++s) {
        if (s + 1 < 20) {
          LAS char* b = W1P(s + 1);
#pragma unroll
          for (int n = 0; n < 7; ++n) bf[(s + 1) & 1][n] = *(LAS v4u*)(b + n * 1024);
        }
        const v4u a0 = a[s % W][0], a1 = a[s % W][1];
#pragma unroll
        for (int j = RAMP(s, W0, W); j < RAMP(s + 1, W0, W); ++j) if (j < 20) A4LD(j)
#pragma unroll
        for (int n = 0; n < 7; ++n) {
          acc[0][n] = mfma(bf[s & 1][n], a0, acc[0][n]);
          acc[1][n] = mfma(bf[s & 1][n], a1, acc[1][n]);
        }
#pragma unroll
        for (int n = 0; n < 7; ++n) { SGB(0x8, 2); SGB(0x100, 1); }
        SCHED();
        if (s == 10 && lane == 0) *(volatile LAS u32*)(L + L_PROG + wave * 4) = 23u;
        if (s == 13) {
#pragma unroll
      for (int m = 0; m < 2; ++m) {
        const float t1 = sum4(__uint_as_float(stv[m].x) + __uint_as_float(stv[m].z));
        const float t2 = sum4(__uint_as_float(stv[m].y) + __uint_as_float(stv[m].w));
        const float mean = t1 * (1.f / 640.f);
        const float var = fmaxf(t2 * (1.f / 640.f) - mean * mean, 0.f);
        mb2[m] = rbf(mean);
        rs2[m] = __builtin_amdgcn_rcpf(rbf(__builtin_amdgcn_sqrtf(rbf(rbf(var) + 1e-5f))));
      }
          asm volatile("" : "+v"(mb2[0]), "+v"(mb2[1]), "+v"(rs2[0]), "+v"(rs2[1]));
        }
      }
      setp(L, wave, lane, 24);
      STAMP(12);
      u32 hp[2][7][2];
#pragma unroll
      for (int n = 0; n < 7; ++n) {
        const f32x4 cs = *(LAS f32x4*)(L + L_CS + (16 * n + 4 * g4) * 4);
#pragma unroll
        for (int m = 0; m < 2; ++m) {
          hp[m][n][0] = pk2(fmaxf((acc[m][n][0] - mb2[m] * cs[0]) * rs2[m], 0.f), fmaxf((acc[m][n][1] - mb2[m] * cs[1]) * rs2[m], 0.f));
          hp[m][n][1] = pk2(fmaxf((acc[m][n][2] - mb2[m] * cs[2]) * rs2[m], 0.f), fmaxf((acc[m][n][3] - mb2[m] * cs[3]) * rs2[m], 0.f));
        }
      }
      const int fbs = WS_FF + ((gi * 8 + 2 * wave) * 54 + (T0 >> 1)) * 1024;
#pragma unroll
      for (int m = 0; m < 2; ++m) {
        const int fb = fbs + m * 54 * 1024;
        if ((T0 & 1) == 0) {
#pragma unroll
          for (int e = 0; e < 3; ++e)
            __builtin_amdgcn_raw_buffer_store_b128(v4u{hp[m][2 * e][0], hp[m][2 * e][1], hp[m][2 * e + 1][0], hp[m][2 * e + 1][1]}, rs, l16, fb + e * 1024, AUX_SC1);
          if (nT == 7) __builtin_amdgcn_raw_buffer_store_b64(v2u{hp[m][6][0], hp[m][6][1]}, rs, l16, fb + 3 * 1024, AUX_SC1);
        } else {
          __builtin_amdgcn_raw_buffer_store_b64(v2u{hp[m][0][0], hp[m][0][1]}, rs, l16 + 8, fb, AUX_SC1);
#pragma unroll
          for (int e = 0; e < 3; ++e)
            __builtin_amdgcn_raw_buffer_store_b128(v4u{hp[m][2 * e + 1][0], hp[m][2 * e + 1][1], hp[m][2 * e + 2][0], hp[m][2 * e + 2][1]}, rs, l16, fb + (e + 1) * 1024, AUX_SC1);
        }
      }
      VMWAIT(0);
      setp(L, wave, lane, 25);
    }
    STAMP(9);
    if (wave == 0) {
      waitp(L, 0, 25);
      if (lane < 16) __hip_atomic_store(inbox + (3 * 256 + gi * 16 + lane) * 16 + gj, tag, RLX_AGENT);
      poll_flags(inbox + (3 * 256 + me) * 16 + (lane & 15), lane < 16, tag);
      setp(L, wave, lane, 26);
    } else waitp1(L, 0, 26);
    STAMP(10);
    {
      const int mh = wave >> 1, kp = wave & 1;
      constexpr int W = 12, W0 = 4;
      v4u a[W][2];
      const int ab = WS_FF + (gi * 8 + 4 * rh + 2 * mh) * 54 * 1024;
#define A5LD(i) { const int o_ = ab + KS5(2 * (i) + kp) * 1024; a[(i) % W][0] = __builtin_amdgcn_raw_buffer_load_b128(rs, l16, o_, AUX_SC1); \
        a[(i) % W][1] = __builtin_amdgcn_raw_buffer_load_b128(rs, l16, o_ + 54 * 1024, AUX_SC1); }
#pragma unroll
      for (int i = 0; i < W0; ++i) A5LD(i)
      SCHED();
      f32x4 acc[2][5];
#pragma unroll
      for (int m = 0; m < 2; ++m)
#pragma unroll
        for (int n = 0; n < 5; ++n) acc[m][n] = zero;
      LAS char* bb = L + L_W2 + kp * 5120 + l16;
      LAS char* b1 = opaque(bb + 12 * 5120);
      LAS char* b2 = opaque(bb + 24 * 5120);
#define W2P(i) ((i) % 15 < 6 ? bb + ((i) % 15) * 10240 : ((i) % 15 < 12 ? b1 + ((i) % 15 - 6) * 10240 : b2 + ((i) % 15 - 12) * 10240))
      waitp(L, 1, 18);
      STAMP(20);
      v4u bf[2][5];
#pragma unroll
      for (int n = 0; n < 5; ++n) bf[0][n] = *(LAS v4u*)(W2P(0) + n * 1024);
#pragma unroll
      for (int i = 0; i < 27; ++i) {
        if (i == 14) waitp(L, 1, 19);
        if (i == 20) waitp(L, 1, 20);
        if (i + 1 < 27) {
#pragma unroll
          for (int n = 0; n < 5; ++n) bf[(i + 1) & 1][n] = *(LAS v4u*)(W2P(i + 1) + n * 1024);
        }
        const v4u a0 = a[i % W][0], a1 = a[i % W][1];
#pragma unroll
        for (int j = RAMP(i, W0, W); j < RAMP(i + 1, W0, W); ++j) if (j < 27) A5LD(j)
#pragma unroll
        for (int n = 0; n < 5; ++n) {
          acc[0][n] = mfma(bf[i & 1][n], a0, acc[0][n]);
          acc[1][n] = mfma(bf[i & 1][n], a1, acc[1][n]);
        }
#pragma unroll
        for (int n = 0; n < 5; ++n) { SGB(0x8, 2); SGB(0x100, 1); }
        SCHED();
        if (i == 5) setp(L, wave, lane, 27);
        if (i == 11) setp(L, wave, lane, 28);
        if (i == 15) setp(L, wave, lane, 29);
      }
      LAS char* xs = L + L_W2 + 24 * 5120 + l16;
      waitp(L, 0, 29);
#pragma unroll
      for (int n = 0; n < 5; ++n) *(LAS f32x4*)(xs + (wave ^ 1) * 5120 + n * 1024) = kp ? acc[0][n] : acc[1][n];
      setp(L, wave, lane, 30);
      waitp1(L, wave ^ 1, 30);
      GAS char* orow = (GAS char*)P.out + (size_t)(128 * gi + 16 * mt3 + tok) * 1280 + ct * 160 + g4 * 8;
#pragma unroll
      for (int n = 0; n < 5; ++n) {
        const f32x4 t = (kp ? acc[1][n] : acc[0][n]) + *(LAS f32x4*)(xs + wave * 5120 + n * 1024);
        v2u o;
        o.x = pk2(bflo(hres[2 * n]) + rbf(t[0]), bfhi(hres[2 * n]) + rbf(t[1]));
        o.y = pk2(bflo(hres[2 * n + 1]) + rbf(t[2]), bfhi(hres[2 * n + 1]) + rbf(t[3]));
        *(GAS v2u*)(orow + n * 32) = o;
      }
    }
  } else {
    {
      v4u wt[3], wa[14];
#pragma unroll
      for (int e = 0; e < 3; ++e) { int u = 90 + v + 4 * e; u = u > 99 ? 99 : u; const int ks = u / 5;
        wt[e] = __builtin_amdgcn_raw_buffer_load_b128(rs, l16, WS_WOT + ct * 5120 + ks * 40960 + (u - 5 * ks) * 1024, AUX_SC1); }
#pragma unroll
      for (int e = 0; e < 14; ++e) { const int u = ROT(v + 4 * e, 56, 28 * (gi & 1)), ks = u / 7;
        wa[e] = __builtin_amdgcn_raw_buffer_load_b128(rs, l16, WS_W1T + (KS4(ks) * 108 + T0 + (u - 7 * ks)) * 1024, AUX_SC1); }
      SCHED();
#pragma unroll
      for (int e = 0; e < 3; ++e) { int u = 90 + v + 4 * e; u = u > 99 ? 99 : u; *(LAS v4u*)(L + L_WO + u * 1024 + l16) = wt[e]; }
      setp(L, wave, lane, 14);
#pragma unroll
      for (int e = 0; e < 14; ++e) *(LAS v4u*)(L + L_W1A + ROT(v + 4 * e, 56, 28 * (gi & 1)) * 1024 + l16) = wa[e];
    }
    if (wave == 7) {
      v4u cp[20];
#pragma unroll
      for (int k = 0; k < 20; ++k) cp[k] = __builtin_amdgcn_raw_buffer_load_b128(rs, (lane < 28 ? lane : 0) * 16, WS_CS1 + (k * 1792 + 16 * T0) * 4, AUX_SC1);
      f32x4 c4 = zero;
#pragma unroll
      for (int k = 0; k < 20; ++k) c4 += __builtin_bit_cast(f32x4, cp[k]);
      if (lane < 28) *(LAS f32x4*)(L + L_CS + lane * 16) = c4;
    }
    STAMP(6);
#define W2LD(u) __builtin_amdgcn_raw_buffer_load_b128(rs, l16, WS_W2T + (KS5((u) / 5) * 40 + 5 * ct + ((u) - 5 * ((u) / 5))) * 1024, AUX_SC1)
    const int w2rot = 2 * (gi & 1) + rh;
    waitps(L, 0, 18);
    {
      v4u pre[21];
#pragma unroll
      for (int e = 0; e < 21; ++e) { int u = v + 4 * e + 42 * (gi & 1); u = u >= 84 ? u - 84 : u; const int ks = u / 7;
        pre[e] = __builtin_amdgcn_raw_buffer_load_b128(rs, l16, WS_W1T + (KS4(8 + ks) * 108 + T0 + (u - 7 * ks)) * 1024, AUX_SC1); }
      SCHED();
      waitp(L, 0, 19);
#pragma unroll
      for (int e = 0; e < 21; ++e) { int u = v + 4 * e + 42 * (gi & 1); u = u >= 84 ? u - 84 : u; *(LAS v4u*)(L + L_W1B + u * 1024 + l16) = pre[e]; }
    }
    setp(L, wave, lane, 15);
    STAMP(7);
    v4u wb[20], wc[25];
    waitp(L, 0, 21);
#pragma unroll
    for (int e = 0; e < 20; ++e) { const int u = ROT(v + 4 * e, 80, 20 * w2rot); wb[e] = W2LD(u); }
    SCHED();
    STAMP(8);
    waitps(L, 0, 23);
#pragma unroll
    for (int e = 0; e < 25; ++e) { const int u = 80 + ROT(v + 4 * e, 100, 0); wc[e] = W2LD(u); }
    SCHED();
    waitp(L, 0, 24);
#pragma unroll
    for (int e = 0; e < 20; ++e) { const int u = ROT(v + 4 * e, 80, 20 * w2rot); *(LAS v4u*)(L + L_W2 + u * 1024 + l16) = wb[e]; }
#pragma unroll
    for (int e = 0; e < 18; ++e) { const int u = 80 + v + 4 * e; if (u < 150) *(LAS v4u*)(L + L_W2 + u * 1024 + l16) = wc[e]; }
    setp(L, wave, lane, 18);
    STAMP(10);
    v4u wd[23];
    waitp(L, 0, 25);
#pragma unroll
    for (int e = 0; e < 23; ++e) { int u = 180 + v + 4 * e; u = u > 269 ? 269 : u; wd[e] = W2LD(u); }
    SCHED();
#define W2PUT(u, val) { *(LAS v4u*)(L + L_W2 + ((u) - 150) * 1024 + l16) = (val); }
    waitp(L, 0, 27);
#pragma unroll
    for (int e = 17; e < 25; ++e) { const int u = 80 + v + 4 * e; if (u >= 150) W2PUT(u, wc[e]) }
#pragma unroll
    for (int e = 0; e < 8; ++e) { const int u = 180 + v + 4 * e; if (u < 210) W2PUT(u, wd[e]) }
    setp(L, wave, lane, 19);
    waitp(L, 0, 28);
#pragma unroll
    for (int e = 7; e < 23; ++e) { const int u = 180 + v + 4 * e; if (u >= 210 && u < 270) W2PUT(u, wd[e]) }
    setp(L, wave, lane, 20);
  }
  STAMP(11);
  asm volatile("fb_code_end:");
}
}

extern "C" void kernel_launch(void* const* d_in, const int* in_sizes, int n_in, void* d_out, int out_size, void* d_ws,
                              size_t ws_size, hipStream_t stream) {
  fb::Params P;
  P.x = (const char*)d_in[0]; P.wqkv = (const char*)d_in[1]; P.wo = (const char*)d_in[2];
  P.w1 = (const char*)d_in[4]; P.w2 = (const char*)d_in[5]; P.out = (char*)d_out; P.ws = (char*)d_ws;
  (void)hipFuncSetAttribute((const void*)fb::fused_block, hipFuncAttributeMaxDynamicSharedMemorySize, fb::LDS_BYTES);
  fb::fused_block<<<256, 512, fb::LDS_BYTES, stream>>>(P);
}
```
